```python
import torch

def _softmax(x, axis=-1):
    return torch.softmax(x, dim=axis)
def _gelu(x):
    return torch.nn.functional.gelu(x, approximate="tanh")
def _silu(x):
    return torch.nn.functional.silu(x)
def _sigmoid(x):
    return torch.sigmoid(x)
def _layernorm(x, axis=-1, eps=1e-5):
    m = x.mean(dim=axis, keepdim=True)
    v = x.var(dim=axis, keepdim=True, unbiased=False)
    return (x - m) / torch.sqrt(v + eps)
def _scatter_add(tgt, idx, src, axis=0):
    return tgt.clone().index_add_(axis, idx, src)
def _segment_sum(src, seg, n):
    out = torch.zeros((n,) + src.shape[1:], dtype=src.dtype)
    return out.index_add_(0, seg, src)
def _pad(x, pw):
    flat = tuple(v for pair in reversed(pw) for v in pair)
    return torch.nn.functional.pad(x, flat)

kernel_name = 'sequential_preLN__compressed_kv_cla__swiglu_ffn__decode'

def setup_inputs():

    _w = (lambda a: (lambda t: t.to(torch.bfloat16) if t.is_floating_point() else t)(torch.as_tensor(a)))
    def _A_setup():
        B, H, Sq, Sk, D, M = 2, 32, 1, 16384, 128, 576
        Dc = 4 * D
        return {
            "x": _w((lambda *s: torch.randn(*s, dtype=torch.bfloat16) * 0.05)(B, Sq, M)),
            "c_kv": _w((lambda *s: torch.randn(*s, dtype=torch.bfloat16) * 0.4)(B, Sk, Dc)),
            "w_q": _w((lambda *s: torch.randn(*s, dtype=torch.bfloat16) * 3.2)(M, H, D) * (M ** -0.5)),
            "w_uk": _w((lambda *s: torch.randn(*s, dtype=torch.bfloat16) * 3.2)(Dc, H, D) * (Dc ** -0.5)),
            "w_uv": _w((lambda *s: torch.randn(*s, dtype=torch.bfloat16) * 0.4)(Dc, H, D) * (Dc ** -0.5)),
            "w_o": _w((lambda *s: torch.randn(*s, dtype=torch.bfloat16) * 0.4)(H, D, M) * ((H * D) ** -0.5)),
        }
    _A_in = _A_setup()

    def _B_setup():
        B, S, D, F = 2, 1, 576, 4608
        return {
            "x": _w((lambda *s: torch.randn(*s, dtype=torch.bfloat16) * 0.05)(B, S, D)),
            "w_gate": _w((lambda *s: torch.randn(*s, dtype=torch.bfloat16) * 0.05)(D, F)),
            "w_up": _w((lambda *s: torch.randn(*s, dtype=torch.bfloat16) * 0.05)(D, F)),
            "w_down": _w((lambda *s: torch.randn(*s, dtype=torch.bfloat16) * 0.05)(F, D)),
        }
    _B_in = _B_setup()

    return {
        "A_x": _A_in["x"],
        "A_c_kv": _A_in["c_kv"],
        "A_w_q": _A_in["w_q"],
        "A_w_uk": _A_in["w_uk"],
        "A_w_uv": _A_in["w_uv"],
        "A_w_o": _A_in["w_o"],
        "B_x": _B_in["x"],
        "B_w_gate": _B_in["w_gate"],
        "B_w_up": _B_in["w_up"],
        "B_w_down": _B_in["w_down"],
    }

def reference(A_x, A_c_kv, A_w_q, A_w_uk, A_w_uv, A_w_o, B_x, B_w_gate, B_w_up, B_w_down):
    def _A_ref(x, c_kv, w_q, w_uk, w_uv, w_o):
        B, Sq, M = x.shape[0], x.shape[1], x.shape[2]
        Sk = c_kv.shape[1]
        H, D = w_q.shape[1], w_q.shape[2]
        q = torch.einsum("bqm,mhd->bhqd", x, w_q)
        k = torch.einsum("bkc,chd->bhkd", c_kv, w_uk)
        v = torch.einsum("bkc,chd->bhkd", c_kv, w_uv)
        scores = torch.einsum("bhqd,bhkd->bhqk", q, k) * (D ** -0.5)
        off = Sk - Sq
        qi = torch.arange(Sq)[:, None]
        kj = torch.arange(Sk)[None, :]
        cmask = kj <= (qi + off)
        scores = torch.where(cmask[None, None], scores, -1e9)
        p = _softmax(scores, axis=-1)
        ctx = torch.einsum("bhqk,bhkd->bhqd", p, v)
        return torch.einsum("bhqd,hdm->bqm", ctx, w_o)

    def _B_ref(x, w_gate, w_up, w_down):
        g = _silu(x @ w_gate)
        u = x @ w_up
        return (g * u) @ w_down

    x = A_x
    h = x + _A_ref(_layernorm(x, axis=-1), A_c_kv, A_w_q, A_w_uk, A_w_uv, A_w_o)
    h2 = h + _B_ref(_layernorm(h, axis=-1), B_w_gate, B_w_up, B_w_down)
    return h2
```

```cpp
#include <hip/hip_runtime.h>
#include <hip/hip_bf16.h>
#include <stdint.h>

typedef unsigned short u16;
typedef short bf16x8 __attribute__((ext_vector_type(8)));
typedef float f32x16 __attribute__((ext_vector_type(16)));
typedef float f32x4 __attribute__((ext_vector_type(4)));
typedef unsigned v4u __attribute__((ext_vector_type(4)));

constexpr int B = 2, H = 32, SK = 16384, D = 128, M = 576, DC = 512, F = 4608;
constexpr int NSPLIT = 128;
constexpr int KPS = SK / NSPLIT;

__device__ __forceinline__ float bf2f(u16 v) { return __uint_as_float((unsigned)v << 16); }
__device__ __forceinline__ u16 f2bf(float f) { __hip_bfloat16 h = __float2bfloat16(f); return *(u16*)&h; }
__device__ __forceinline__ float wave_sum(float v) { for (int o = 32; o; o >>= 1) v += __shfl_xor(v, o); return v; }
__device__ __forceinline__ float wave_max(float v) { for (int o = 32; o; o >>= 1) v = fmaxf(v, __shfl_xor(v, o)); return v; }

__device__ void ln2(const float* src, float* dst, float* red) {
    int tid = threadIdx.x, w = tid >> 6, lane = tid & 63;
    if (w < 2) {
        float s = 0.f;
        for (int i = lane; i < M; i += 64) s += src[w * M + i];
        s = wave_sum(s); float mu = s / M;
        float v = 0.f;
        for (int i = lane; i < M; i += 64) { float d = src[w * M + i] - mu; v += d * d; }
        v = wave_sum(v); float rs = 1.0f / sqrtf(v / M + 1e-5f);
        for (int i = lane; i < M; i += 64) dst[w * M + i] = (src[w * M + i] - mu) * rs;
    }
    __syncthreads();
}

__global__ __launch_bounds__(256) void k_q(const u16* x, const u16* wq, float* q) {
    __shared__ float xs[2 * M], xn[2 * M], red[2 * 16 * 128];
    int tid = threadIdx.x;
    for (int i = tid; i < 2 * M; i += 256) xs[i] = bf2f(x[i]);
    __syncthreads();
    ln2(xs, xn, red);
    int cg = tid & 1, rg = tid >> 1;
    int col0 = blockIdx.x * 16 + cg * 8;
    float a0[8] = {0}, a1[8] = {0};
    for (int r = rg; r < M; r += 128) {
        v4u w = *(const v4u*)(wq + (size_t)r * 4096 + col0);
        float x0 = xn[r], x1 = xn[M + r];
        for (int j = 0; j < 4; ++j) {
            float lo = __uint_as_float(w[j] << 16), hi = __uint_as_float(w[j] & 0xffff0000u);
            a0[2 * j] += x0 * lo; a0[2 * j + 1] += x0 * hi; a1[2 * j] += x1 * lo; a1[2 * j + 1] += x1 * hi;
        }
    }
    for (int j = 0; j < 8; ++j) { red[(0 * 16 + cg * 8 + j) * 128 + rg] = a0[j]; red[(16 + cg * 8 + j) * 128 + rg] = a1[j]; }
    __syncthreads();
    if (tid < 32) { float s = 0.f; for (int i = 0; i < 128; ++i) s += red[tid * 128 + i]; int b = tid >> 4, c = tid & 15; q[b * 4096 + blockIdx.x * 16 + c] = s; }
}

__global__ __launch_bounds__(256) void k_abs(const float* q, const u16* wuk, u16* qa) {
    int h = blockIdx.x >> 3, j = blockIdx.x & 7, tid = threadIdx.x;
    int l16 = tid & 15, cr = tid >> 4;
    float q0[8], q1[8];
    for (int i = 0; i < 8; ++i) { q0[i] = q[h * 128 + l16 * 8 + i]; q1[i] = q[4096 + h * 128 + l16 * 8 + i]; }
    const float sc = 0.08838834764831845f * 1.4426950408889634f;
    for (int it = 0; it < 4; ++it) {
        int c = j * 64 + it * 16 + cr;
        v4u w = *(const v4u*)(wuk + ((size_t)c * 32 + h) * 128 + l16 * 8);
        float s0 = 0.f, s1 = 0.f;
        for (int k = 0; k < 4; ++k) {
            float lo = __uint_as_float(w[k] << 16), hi = __uint_as_float(w[k] & 0xffff0000u);
            s0 += q0[2 * k] * lo + q0[2 * k + 1] * hi; s1 += q1[2 * k] * lo + q1[2 * k + 1] * hi;
        }
        for (int o = 8; o; o >>= 1) { s0 += __shfl_xor(s0, o); s1 += __shfl_xor(s1, o); }
        if (l16 == 0) { qa[(0 * 32 + h) * 512 + c] = f2bf(s0 * sc); qa[(32 + h) * 512 + c] = f2bf(s1 * sc); }
    }
}

__global__ __launch_bounds__(256) void k_attn(const u16* ckv, const u16* qa, float* po, float* pm, float* pl) {
    extern __shared__ __attribute__((aligned(16))) char smem[];
    u16* ks = (u16*)smem;
    constexpr int LDK = 520;
    u16* ps = ks + 128 * LDK;
    constexpr int LDP = 136;
    float* wm = (float*)(ps + 32 * LDP);
    float* wl = wm + 128;
    int b = blockIdx.x / NSPLIT, sp = blockIdx.x % NSPLIT, tid = threadIdx.x, w = tid >> 6, lane = tid & 63;
    const u16* kg = ckv + ((size_t)b * SK + sp * KPS) * DC;
    for (int i = tid; i < 128 * 64; i += 256) { int r = i >> 6, c = i & 63; *(v4u*)(ks + r * LDK + c * 8) = *(const v4u*)(kg + (size_t)r * DC + c * 8); }
    __syncthreads();
    int r = lane & 31, hh = lane >> 5;
    f32x16 s; for (int i = 0; i < 16; ++i) s[i] = 0.f;
    for (int st = 0; st < 32; ++st) {
        bf16x8 a = *(const bf16x8*)(ks + (32 * w + r) * LDK + 16 * st + 8 * hh);
        bf16x8 bq = *(const bf16x8*)(qa + ((size_t)b * 32 + r) * 512 + 16 * st + 8 * hh);
        s = __builtin_amdgcn_mfma_f32_32x32x16_bf16(a, bq, s, 0, 0, 0);
    }
    float mx = s[0]; for (int i = 1; i < 16; ++i) mx = fmaxf(mx, s[i]);
    mx = fmaxf(mx, __shfl_xor(mx, 32));
    if (lane < 32) wm[w * 32 + lane] = mx;
    __syncthreads();
    float gm = fmaxf(fmaxf(wm[r], wm[32 + r]), fmaxf(wm[64 + r], wm[96 + r]));
    float ls = 0.f;
    for (int i = 0; i < 16; ++i) {
        float p = exp2f(s[i] - gm); u16 pb = f2bf(p); ls += bf2f(pb);
        int key = 32 * w + (i & 3) + 8 * (i >> 2) + 4 * hh;
        ps[r * LDP + key] = pb;
    }
    ls += __shfl_xor(ls, 32);
    if (lane < 32) wl[w * 32 + lane] = ls;
    __syncthreads();
    for (int t = 0; t < 4; ++t) {
        f32x16 o; for (int i = 0; i < 16; ++i) o[i] = 0.f;
        int c = 128 * w + 32 * t + r;
        for (int st = 0; st < 8; ++st) {
            bf16x8 a = *(const bf16x8*)(ps + r * LDP + 16 * st + 8 * hh);
            bf16x8 bv; for (int j = 0; j < 8; ++j) bv[j] = (short)ks[(16 * st + 8 * hh + j) * LDK + c];
            o = __builtin_amdgcn_mfma_f32_32x32x16_bf16(a, bv, o, 0, 0, 0);
        }
        for (int i = 0; i < 16; ++i) { int head = (i & 3) + 8 * (i >> 2) + 4 * hh; po[(((size_t)b * 32 + head) * NSPLIT + sp) * 512 + c] = o[i]; }
    }
    if (tid < 32) { pm[((size_t)b * 32 + tid) * NSPLIT + sp] = fmaxf(fmaxf(wm[tid], wm[32 + tid]), fmaxf(wm[64 + tid], wm[96 + tid])); pl[((size_t)b * 32 + tid) * NSPLIT + sp] = wl[tid] + wl[32 + tid] + wl[64 + tid] + wl[96 + tid]; }
}

__global__ __launch_bounds__(256) void k_comb(const float* po, const float* pm, const float* pl, const u16* wuv, float* ctxp) {
    __shared__ float wgt[2][128], lat[2][64], red[2][4][64], r2[2][2][128];
    int h = blockIdx.x >> 3, j = blockIdx.x & 7, tid = threadIdx.x, w = tid >> 6, lane = tid & 63;
    if (w < 2) {
        int b = w; const float* m = pm + ((size_t)b * 32 + h) * NSPLIT; const float* l = pl + ((size_t)b * 32 + h) * NSPLIT;
        float m0 = m[lane], m1 = m[lane + 64]; float gm = wave_max(fmaxf(m0, m1));
        float e0 = exp2f(m0 - gm), e1 = exp2f(m1 - gm); float L = wave_sum(e0 * l[lane] + e1 * l[lane + 64]);
        wgt[b][lane] = e0 / L; wgt[b][lane + 64] = e1 / L;
    }
    __syncthreads();
    for (int b = 0; b < 2; ++b) {
        float a = 0.f; const float* p = po + (((size_t)b * 32 + h) * NSPLIT) * 512 + j * 64 + lane;
        for (int sp = w; sp < NSPLIT; sp += 4) a += wgt[b][sp] * p[(size_t)sp * 512];
        red[b][w][lane] = a;
    }
    __syncthreads();
    if (tid < 128) { int b = tid >> 6; lat[b][lane] = red[b][0][lane] + red[b][1][lane] + red[b][2][lane] + red[b][3][lane]; }
    __syncthreads();
    int d = tid & 127, half = tid >> 7; float a0 = 0.f, a1 = 0.f;
    for (int c = half * 32; c < half * 32 + 32; ++c) { float wv = bf2f(wuv[((size_t)(j * 64 + c) * 32 + h) * 128 + d]); a0 += lat[0][c] * wv; a1 += lat[1][c] * wv; }
    r2[0][half][d] = a0; r2[1][half][d] = a1;
    __syncthreads();
    { int b = tid >> 7; ctxp[((size_t)j * 2 + b) * 4096 + h * 128 + d] = r2[b][0][d] + r2[b][1][d]; }
}

__global__ __launch_bounds__(256) void k_wo(const float* ctxp, const u16* wo, float* op) {
    __shared__ float cx[2][16];
    int tid = threadIdx.x, i = blockIdx.x;
    if (tid < 32) { int b = tid >> 4, k = tid & 15; float s = 0.f; for (int j = 0; j < 8; ++j) s += ctxp[((size_t)j * 2 + b) * 4096 + i * 16 + k]; cx[b][k] = s; }
    __syncthreads();
    for (int m = tid; m < M; m += 256) { float a0 = 0.f, a1 = 0.f; for (int k = 0; k < 16; ++k) { float wv = bf2f(wo[(size_t)(i * 16 + k) * M + m]); a0 += cx[0][k] * wv; a1 += cx[1][k] * wv; } op[((size_t)i * 2) * M + m] = a0; op[((size_t)i * 2 + 1) * M + m] = a1; }
}
__global__ __launch_bounds__(256) void k_red(const float* op, const u16* x, const float* hbase, float* hout, u16* obf) {
    __shared__ float red[4][64];
    int tid = threadIdx.x, w = tid >> 6, lane = tid & 63; int col = blockIdx.x * 64 + lane;
    float a = 0.f; for (int i = w; i < 256; i += 4) a += op[(size_t)i * 1152 + col];
    red[w][lane] = a; __syncthreads();
    if (w == 0) { float s = red[0][lane] + red[1][lane] + red[2][lane] + red[3][lane]; float base = x ? bf2f(x[col]) : hbase[col]; s += base; if (hout) hout[col] = s; if (obf) obf[col] = f2bf(s); }
}
__global__ __launch_bounds__(256) void k_ffn(const float* hh, const u16* wg, const u16* wu, const u16* wd, float* op) {
    __shared__ float hs[2 * M], hn[2 * M], red[4][14][18], act[2][18];
    int tid = threadIdx.x, i = blockIdx.x;
    for (int k = tid; k < 2 * M; k += 256) hs[k] = hh[k];
    __syncthreads();
    ln2(hs, hn, nullptr);
    int f = tid % 18, rg = tid / 18;
    if (rg < 14) {
        float g0 = 0, g1 = 0, u0 = 0, u1 = 0;
        for (int r = rg; r < M; r += 14) { float a = bf2f(wg[(size_t)r * F + i * 18 + f]), c = bf2f(wu[(size_t)r * F + i * 18 + f]); g0 += hn[r] * a; g1 += hn[M + r] * a; u0 += hn[r] * c; u1 += hn[M + r] * c; }
        red[0][rg][f] = g0; red[1][rg][f] = g1; red[2][rg][f] = u0; red[3][rg][f] = u1;
    }
    __syncthreads();
    if (tid < 36) { int b = tid / 18, ff = tid % 18; float g = 0, u = 0; for (int r = 0; r < 14; ++r) { g += red[b][r][ff]; u += red[2 + b][r][ff]; } act[b][ff] = g / (1.f + __expf(-g)) * u; }
    __syncthreads();
    for (int m = tid; m < M; m += 256) { float a0 = 0, a1 = 0; for (int k = 0; k < 18; ++k) { float wv = bf2f(wd[(size_t)(i * 18 + k) * M + m]); a0 += act[0][k] * wv; a1 += act[1][k] * wv; } op[((size_t)i * 2) * M + m] = a0; op[((size_t)i * 2 + 1) * M + m] = a1; }
}

extern "C" void kernel_launch(void* const* d_in, const int* in_sizes, int n_in, void* d_out, int out_size, void* d_ws, size_t ws_size, hipStream_t stream) {
    const u16* x = (const u16*)d_in[0]; const u16* ckv = (const u16*)d_in[1]; const u16* wq = (const u16*)d_in[2]; const u16* wuk = (const u16*)d_in[3];
    const u16* wuv = (const u16*)d_in[4]; const u16* wo = (const u16*)d_in[5]; const u16* wg = (const u16*)d_in[7]; const u16* wu = (const u16*)d_in[8]; const u16* wd = (const u16*)d_in[9];
    char* ws = (char*)d_ws;
    float* q = (float*)ws; ws += 2 * 4096 * 4;
    u16* qa = (u16*)ws; ws += 2 * 32 * 512 * 2;
    float* pm = (float*)ws; ws += 2 * 32 * NSPLIT * 4;
    float* pl = (float*)ws; ws += 2 * 32 * NSPLIT * 4;
    float* ctxp = (float*)ws; ws += 8 * 2 * 4096 * 4;
    float* op = (float*)ws; ws += 256 * 1152 * 4;
    float* hh = (float*)ws; ws += 1152 * 4 + 512;
    float* po = (float*)ws;
    k_q<<<256, 256, 0, stream>>>(x, wq, q);
    k_abs<<<256, 256, 0, stream>>>(q, wuk, qa);
    size_t sm = 128 * 520 * 2 + 32 * 136 * 2 + 256 * 4;
    hipFuncSetAttribute((const void*)k_attn, hipFuncAttributeMaxDynamicSharedMemorySize, (int)sm);
    k_attn<<<256, 256, sm, stream>>>(ckv, qa, po, pm, pl);
    k_comb<<<256, 256, 0, stream>>>(po, pm, pl, wuv, ctxp);
    k_wo<<<256, 256, 0, stream>>>(ctxp, wo, op);
    k_red<<<18, 256, 0, stream>>>(op, x, nullptr, hh, nullptr);
    k_ffn<<<256, 256, 0, stream>>>(hh, wg, wu, wd, op);
    k_red<<<18, 256, 0, stream>>>(op, nullptr, hh, nullptr, (u16*)d_out);
}
```

```cpp
#include <hip/hip_runtime.h>
#include <hip/hip_bf16.h>
#include <stdint.h>

typedef unsigned short u16;
typedef unsigned long long u64;
typedef short bf16x8 __attribute__((ext_vector_type(8)));
typedef short s16x4 __attribute__((ext_vector_type(4)));
typedef float f32x4 __attribute__((ext_vector_type(4)));
typedef float f32x16 __attribute__((ext_vector_type(16)));
typedef unsigned v4u __attribute__((ext_vector_type(4)));
typedef unsigned v2u __attribute__((ext_vector_type(2)));
#define LAS __attribute__((address_space(3)))
#define GAS __attribute__((address_space(1)))
#define DI __device__ __forceinline__

#define STAMPS 0
#define NA_ROWS 0
#define NB_ROWS 32

constexpr int SK = 16384, MD = 576, DC = 512, FF = 4608;
constexpr int NT = 512;
constexpr int LDS_BYTES = 163840;
constexpr int AUX = 131072;
constexpr unsigned WS_EPOCH = 0;
constexpr unsigned WS_FLAG = 4096;
constexpr unsigned WS_E1 = 32768;
constexpr unsigned WS_QA = WS_E1 + 262144;
constexpr unsigned WS_CLR = WS_QA + 65536;
constexpr unsigned WS_ST = WS_CLR;
constexpr unsigned WS_E4 = WS_ST + 262144;
constexpr unsigned WS_E5 = WS_E4 + 131072;
constexpr unsigned WS_E7 = WS_E5 + 589824;
constexpr unsigned WS_H = WS_E7 + 589824;
constexpr unsigned WS_CLR_END = WS_H + 65536 + 65536;
constexpr unsigned CLR_PER = (WS_CLR_END - WS_CLR) / 128;
static_assert(CLR_PER * 128 == WS_CLR_END - WS_CLR && CLR_PER % 1024 == 0, "clr");
constexpr unsigned WS_PO = 4194304;
constexpr unsigned WS_DBG = WS_PO + 8388608;
constexpr unsigned SENT = 0xFFFFFFFFu;
constexpr int SPIN_CAP = 400000;

DI float bf2f(u16 v) { return __uint_as_float((unsigned)v << 16); }
DI unsigned pk2(float a, float b) {
    typedef __bf16 bf2 __attribute__((ext_vector_type(2)));
    typedef float f2 __attribute__((ext_vector_type(2)));
    f2 v = {a, b}; bf2 r = __builtin_convertvector(v, bf2); return __builtin_bit_cast(unsigned, r);
}
DI float lo_f(unsigned p) { return __uint_as_float(p << 16); }
DI float hi_f(unsigned p) { return __uint_as_float(p & 0xffff0000u); }
DI unsigned nosent(float v) { return __builtin_elementwise_min(__float_as_uint(v), 0xFFFFFFFEu); }
template <class T> DI void pin(T& v) { asm volatile("" : "+v"(v)); }
DI float ex2(float v) { return __builtin_amdgcn_exp2f(v); }
#define NC_ROWS 32
#define E1_SLEEP 2
#define TOUCH 1
#define G1_EARLYW 1
#define FFN_SLEEP 0
#define FFN_AUX 0
#define SWAP 0
#define NF_EARLY 16
#define NW_EARLY 24
#define NA1 16
#define NA2 24
#define NB1 48
#define NB2 24
#define G1_SLEEP 0
#define G1_N0 1
#define G1_N1 4
#define G1_F1 1
#define G1_F2 5
DI int sw(int row) { return ((row & 3) << 2) | ((row >> 2) & 3); }
DI void bar() { asm volatile("s_waitcnt lgkmcnt(0)\n\ts_barrier" ::: "memory"); }
DI void glds(const void* g, unsigned lds_, int mode) {
    const unsigned lds = __builtin_amdgcn_readfirstlane(lds_);
    unsigned keep;
    if (mode == 1) asm volatile("s_mov_b32 %0, m0\n\ts_mov_b32 m0, %2\n\ts_nop 0\n\tglobal_load_lds_dwordx4 %1, off nt\n\ts_mov_b32 m0, %0" : "=&s"(keep) : "v"(g), "s"(lds) : "memory");
    else if (mode == 2) asm volatile("s_mov_b32 %0, m0\n\ts_mov_b32 m0, %2\n\ts_nop 0\n\tglobal_load_lds_dwordx4 %1, off sc1\n\ts_mov_b32 m0, %0" : "=&s"(keep) : "v"(g), "s"(lds) : "memory");
    else asm volatile("s_mov_b32 %0, m0\n\ts_mov_b32 m0, %2\n\ts_nop 0\n\tglobal_load_lds_dwordx4 %1, off\n\ts_mov_b32 m0, %0" : "=&s"(keep) : "v"(g), "s"(lds) : "memory");
}
DI void vm0() { asm volatile("s_waitcnt vmcnt(0)" ::: "memory"); }
DI v4u ld16(__amdgpu_buffer_rsrc_t rs, unsigned off) { return __builtin_amdgcn_raw_buffer_load_b128(rs, off, 0, 16); }
DI v2u ld8(__amdgpu_buffer_rsrc_t rs, unsigned off) { return __builtin_amdgcn_raw_buffer_load_b64(rs, off, 0, 16); }
DI void st16(__amdgpu_buffer_rsrc_t rs, unsigned off, v4u v) { __builtin_amdgcn_raw_buffer_store_b128(v, rs, off, 0, 16); }
DI void st8(__amdgpu_buffer_rsrc_t rs, unsigned off, v2u v) { __builtin_amdgcn_raw_buffer_store_b64(v, rs, off, 0, 16); }
DI bool ok4(v4u v) { return v[0] != SENT && v[1] != SENT && v[2] != SENT && v[3] != SENT; }
DI f32x4 mfma16(bf16x8 a, bf16x8 b, f32x4 c) { return __builtin_amdgcn_mfma_f32_16x16x32_bf16(a, b, c, 0, 0, 0); }
template <int P> DI float dpp(float v) { return __int_as_float(__builtin_amdgcn_mov_dpp(__float_as_int(v), P, 0xf, 0xf, true)); }
DI float sum16(float v) { auto r = __builtin_amdgcn_permlane16_swap(__float_as_uint(v), __float_as_uint(v), false, false); return __uint_as_float(r[0]) + __uint_as_float(r[1]); }
DI float sum32(float v) { auto r = __builtin_amdgcn_permlane32_swap(__float_as_uint(v), __float_as_uint(v), false, false); return __uint_as_float(r[0]) + __uint_as_float(r[1]); }
DI float max16(float v) { auto r = __builtin_amdgcn_permlane16_swap(__float_as_uint(v), __float_as_uint(v), false, false); return fmaxf(__uint_as_float(r[0]), __uint_as_float(r[1])); }
DI float max32(float v) { auto r = __builtin_amdgcn_permlane32_swap(__float_as_uint(v), __float_as_uint(v), false, false); return fmaxf(__uint_as_float(r[0]), __uint_as_float(r[1])); }
DI float rowsum(float v) { v += dpp<0x128>(v); v += dpp<0x124>(v); v += dpp<0x122>(v); v += dpp<0x121>(v); return v; }
DI float rowmax(float v) { v = fmaxf(v, dpp<0x128>(v)); v = fmaxf(v, dpp<0x124>(v)); v = fmaxf(v, dpp<0x122>(v)); v = fmaxf(v, dpp<0x121>(v)); return v; }
DI float halfsum(float v) { return sum16(rowsum(v)); }
DI float other_half(float v) { auto r = __builtin_amdgcn_permlane32_swap(__float_as_uint(v), __float_as_uint(v), false, false); return __uint_as_float((threadIdx.x & 32) ? r[0] : r[1]); }
DI f32x16 mfma32(bf16x8 a, bf16x8 b, f32x16 c) { return __builtin_amdgcn_mfma_f32_32x32x16_bf16(a, b, c, 0, 0, 0); }
DI bf16x8 trfrag(LAS char* L, unsigned a0, unsigned a1) {
    s16x4 x = __builtin_amdgcn_ds_read_tr16_b64_v4i16((LAS s16x4*)(L + a0));
    s16x4 y = __builtin_amdgcn_ds_read_tr16_b64_v4i16((LAS s16x4*)(L + a1));
    return __builtin_shufflevector(x, y, 0, 1, 2, 3, 4, 5, 6, 7);
}
DI bf16x8 asb(v4u v) { return __builtin_bit_cast(bf16x8, v); }
DI f32x4 fold(f32x4 d) { return f32x4{d[0] + dpp<0xEE>(d[0]), d[1] + dpp<0xEE>(d[1]), d[2] + dpp<0xEE>(d[2]), d[3] + dpp<0xEE>(d[3])}; }
DI v4u nos4(f32x4 d) { return v4u{nosent(d[0]), nosent(d[1]), nosent(d[2]), nosent(d[3])}; }
DI v4u split8(const float* n, bool lo_part) { v4u o;
#pragma unroll
    for (int j = 0; j < 4; ++j) { unsigned hi = pk2(n[2 * j], n[2 * j + 1]); o[j] = lo_part ? pk2(n[2 * j] - lo_f(hi), n[2 * j + 1] - hi_f(hi)) : hi; }
    return o; }
template <int N> DI void vmw() { asm volatile("s_waitcnt vmcnt(%0)" :: "n"(N) : "memory"); }

#define STAMP(i) do {} while (0)
#define SB() __builtin_amdgcn_sched_barrier(0)

__global__ __launch_bounds__(NT, 2) void mk(const u16* __restrict__ x, const u16* __restrict__ ckv, const u16* __restrict__ wq, const u16* __restrict__ wuk,
                                           const u16* __restrict__ wuv, const u16* __restrict__ wo, const u16* __restrict__ wg, const u16* __restrict__ wu,
                                           const u16* __restrict__ wd, u16* __restrict__ out, char* __restrict__ ws) {
    extern __shared__ __attribute__((aligned(16))) char smem_[];
    LAS char* L = (LAS char*)smem_;
    const int tid = threadIdx.x, lane = tid & 63, wave = __builtin_amdgcn_readfirstlane(tid >> 6);
    const int g = lane >> 4, l15 = lane & 15, l31 = lane & 31, hf = lane >> 5;
    const int q4 = l15 >> 2, p4 = lane & 3, b1 = lane & 1;
    const int bid = blockIdx.x, xc = bid & 7, jj = bid >> 3;
    const bool isQ = jj < 16;
    const int idx = jj & 15, a = xc * 16 + idx;
    const int hh = xc * 4 + (idx >> 2), rr = idx & 3;
    const int bb = a >> 6, split = (a & 63) + (isQ ? 64 : 0);
    const __amdgpu_buffer_rsrc_t rs = __builtin_amdgcn_make_buffer_rsrc(ws, 0, 0x10000000, 0x00020000);
    const u16* kvg = ckv + ((size_t)bb * SK + split * 128) * DC;
    const f32x4 z4 = {0.f, 0.f, 0.f, 0.f};
    STAMP(0);
    auto dma_kv = [&](int row) { glds(kvg + (size_t)row * DC + ((lane ^ sw(row)) << 3), (unsigned)row * 1024u, 1); };
    unsigned t0a, t0b;
    { int ra = 8 * g + q4, rb = ra + 4, ch = 2 * wave + (p4 >> 1); t0a = ra * 256 + ((ch ^ sw(ra)) << 4) + 8 * (p4 & 1); t0b = rb * 256 + ((ch ^ sw(rb)) << 4) + 8 * (p4 & 1); }
    unsigned tag0, tag1;
    constexpr unsigned XRAW = AUX + 20480, EPL = AUX + 24576, QIMG = AUX + 26624, WQI = 88 * 1024, WKI = 96 * 1024;
    if (!isQ) {
        const int nk = rr < 2 ? 5 : 4, row0 = rr < 2 ? 160 * rr : 64 + 128 * rr;
        v4u xf[5], xs[3]; v2u ep;
        if (wave == 7) {
            glds(x + lane * 8, XRAW, 0); glds(x + 512 + lane * 8, XRAW + 1024, 0); glds(x + 1024 + (lane & 15) * 8, XRAW + 2048, 0);
            glds(ws + WS_EPOCH + lane * 16, EPL, 2);
        }
        for (int n = wave; n < 8 * nk; n += 8) { int row = 4 * n + g; glds(wq + (size_t)(row0 + row) * 4096 + hh * 128 + ((l15 ^ sw(row)) << 3), WQI + n * 1024, 1); }
#pragma unroll
        for (int i = 0; i < NA1 / 8; ++i) dma_kv(wave + 8 * i);
        unsigned e1w = WS_E1 + ((hh * 4 + rr) * 2 + b1) * 1024 + (16 * wave + 4 * g + (l15 & 2)) * 8;
        unsigned wa = WQI + t0a, wb = WQI + t0b;
        pin(e1w); pin(wa); pin(wb);
        if (wave == 7) vmw<4 + NA1 / 8>();
        bar();
        STAMP(8);
        ep = *(LAS v2u*)(L + EPL);
        xs[0] = *(LAS v4u*)(L + XRAW + hf * 1152 + l31 * 16); xs[1] = *(LAS v4u*)(L + XRAW + hf * 1152 + 512 + l31 * 16); xs[2] = *(LAS v4u*)(L + XRAW + hf * 1152 + 1024 + (l31 & 7) * 16);
#pragma unroll
        for (int ks = 0; ks < 5; ++ks) xf[ks] = *(LAS v4u*)(L + XRAW + b1 * 1152 + (row0 + 32 * (ks < nk ? ks : 0) + 8 * g) * 2);
        { u64 t = (((u64)ep[1] << 32) | ep[0]) + 1ull; tag0 = (unsigned)t; tag1 = (unsigned)(t >> 32); }
        float mu, rsd;
        {
            const float ok2 = l31 < 8 ? 1.f : 0.f;
            float v[24]; float s = 0.f;
#pragma unroll
            for (int i = 0; i < 3; ++i)
#pragma unroll
                for (int j = 0; j < 4; ++j) { v[8 * i + 2 * j] = lo_f(xs[i][j]); v[8 * i + 2 * j + 1] = hi_f(xs[i][j]); }
            float s2 = 0.f;
#pragma unroll
            for (int i = 0; i < 16; ++i) s += v[i];
#pragma unroll
            for (int i = 16; i < 24; ++i) s2 += v[i];
            mu = halfsum(s + ok2 * s2) * (1.f / 576.f); float q = 0.f, q2 = 0.f;
#pragma unroll
            for (int i = 0; i < 16; ++i) { float d = v[i] - mu; q += d * d; }
#pragma unroll
            for (int i = 16; i < 24; ++i) { float d = v[i] - mu; q2 += d * d; }
            rsd = __builtin_amdgcn_rsqf(halfsum(q + ok2 * q2) * (1.f / 576.f) + 1e-5f);
            float mo = other_half(mu), ro = other_half(rsd);
            if (b1 != hf) { mu = mo; rsd = ro; }
            if (l15 == 2) {
#pragma unroll
                for (int ks = 0; ks < 5; ++ks) xf[ks] = v4u{0x3F803F80u, 0x3F803F80u, 0x3F803F80u, 0x3F803F80u}; }
#pragma unroll
            for (int ks = 0; ks < 5; ++ks) pin(xf[ks]);
        }
        unsigned t40lo = tag0 << 24, t40hi = (tag0 >> 8) | (tag1 << 24);
        pin(t40lo); pin(t40hi); pin(mu); pin(rsd);
        vmw<NA1 / 8>();
        bar();
        STAMP(1);
        { f32x4 d = z4;
#pragma unroll
          for (int ks = 0; ks < 4; ++ks) d = mfma16(trfrag(L, wa + ks * 8192, wb + ks * 8192), asb(xf[ks]), d);
          if (nk == 5) d = mfma16(trfrag(L, wa + 32768, wb + 32768), asb(xf[4]), d);
#pragma unroll
          for (int j = 0; j < 4; ++j) d[j] = (d[j] - mu * dpp<0xAA>(d[j])) * rsd;
          float e0 = dpp<0x44>(d[2]), e1 = dpp<0x44>(d[3]);
          if (l15 & 2) { d[0] = e0; d[1] = e1; }
          if (l15 < 4) st16(rs, e1w, v4u{((__float_as_uint(d[0]) + 0x80u) >> 8) | t40lo, t40hi, ((__float_as_uint(d[1]) + 0x80u) >> 8) | t40lo, t40hi}); }
        bar();
        STAMP(2);
#pragma unroll
        for (int i = NA1 / 8; i < 16; ++i) { dma_kv(wave + 8 * i); if (i >= G1_F2) vmw<G1_F2>(); }
        vm0();
        STAMP(3);
    } else {
        if (wave == 7) glds(ws + WS_EPOCH + lane * 16, EPL, 2);
        if (wave == 6) { const char* tp = lane < 8 ? ws + WS_FLAG + lane * 1024 + (a >> 4) * 128 : ws + WS_QA + (((lane & 1) * 32 + hh) * 512 + 128 * rr + ((lane >> 1) & 1) * 64) * 2; glds(tp, EPL + 3072, 0); }
        for (int n = wave; n < 32; n += 8) { int row = 4 * n + g; glds(wuk + ((size_t)(128 * rr + row) * 32 + hh) * 128 + ((l15 ^ sw(row)) << 3), WKI + n * 1024, 1); }
        { v4u s4 = {SENT, SENT, SENT, SENT};
          st16(rs, WS_CLR + a * CLR_PER + wave * 1024 + lane * 16, s4);
          st16(rs, WS_CLR + a * CLR_PER + 8192 + (wave < 5 ? wave : 0) * 1024 + lane * 16, s4); }
#pragma unroll
        for (int i = 0; i < NA2 / 8; ++i) dma_kv(wave + 8 * i);
        unsigned e1r = WS_E1 + ((hh * 4 + g) * 2 + (l15 >> 3)) * 1024 + (16 * wave + 2 * (lane & 7)) * 8;
        unsigned qw = QIMG + (l15 >> 3) * 272 + (16 * wave + 2 * (lane & 7)) * 2;
        unsigned ka0, ka1, qr, qaw;
        { const int c0 = 32 * (wave & 3) + 8 * q4 + (l15 & 3), c1 = c0 + 4; ka0 = WKI + c0 * 256 + ((g ^ sw(c0)) << 4); ka1 = WKI + c1 * 256 + ((g ^ sw(c1)) << 4);
          qr = QIMG + b1 * 272 + 16 * g; qaw = WS_QA + ((b1 * 32 + hh) * 512 + 128 * rr + 32 * (wave & 3) + 8 * g) * 2; }
        pin(e1r); pin(qw); pin(ka0); pin(ka1); pin(qr); pin(qaw);
        vm0();
        bar();
        STAMP(1);
        { v2u ep = *(LAS v2u*)(L + EPL); u64 t = (((u64)ep[1] << 32) | ep[0]) + 1ull; tag0 = (unsigned)t; tag1 = (unsigned)(t >> 32); }
        const unsigned t40lo = tag0 << 24, t40hi = (tag0 >> 8) | (tag1 << 24);
        { v4u v; bool okv = false;
          for (int sp = 0; sp < SPIN_CAP; ++sp) { asm volatile("" ::: "memory");
              if (!okv) v = ld16(rs, e1r);
              okv = (v[1] == t40hi) & (v[3] == t40hi) & ((v[0] & 0xFF000000u) == t40lo) & ((v[2] & 0xFF000000u) == t40lo);
              if (__all(okv)) break; if (E1_SLEEP) __builtin_amdgcn_s_sleep(E1_SLEEP); }
          float q0 = sum32(sum16(__uint_as_float(v[0] << 8))), q1 = sum32(sum16(__uint_as_float(v[2] << 8)));
          if (lane < 16) *(LAS unsigned*)(L + qw) = pk2(q0, q1);
        }
        bar();
        STAMP(2);
        if (wave < 4) {
            f32x4 d0 = z4, d1 = z4;
#pragma unroll
            for (int ks = 0; ks < 4; ++ks) { bf16x8 bq = *(LAS bf16x8*)(L + qr + 64 * ks);
                d0 = mfma16(*(LAS bf16x8*)(L + (ka0 ^ (ks << 6))), bq, d0); d1 = mfma16(*(LAS bf16x8*)(L + (ka1 ^ (ks << 6))), bq, d1); }
            const float sc = 0.08838834764831845f * 1.4426950408889634f;
            d0 *= sc; d1 *= sc;
            if (l15 < 2) st16(rs, qaw, v4u{pk2(d0[0], d0[1]), pk2(d0[2], d0[3]), pk2(d1[0], d1[1]), pk2(d1[2], d1[3])});
            vm0();
        }
        bar();
        if (wave == 0 && lane < 8) st8(rs, WS_FLAG + lane * 1024 + a * 8, v2u{tag0, tag1});
        if (wave >= 4) for (int r = NA2 + wave - 4; r < NA2 + NB2; r += 4) dma_kv(r);
        STAMP(3);
    }
    const int NAr = isQ ? NA2 : 128, NBr = isQ ? NB2 : 0;
    unsigned a8[8];
#pragma unroll
    for (int j = 0; j < 8; ++j) { a8[j] = AUX + l31 * 1024 + (((2 * j + hf) ^ sw(l31)) << 4); pin(a8[j]); }
    if (wave == 0) {
        for (int sp = 0; sp < SPIN_CAP; ++sp) { asm volatile("" ::: "memory"); v4u f = ld16(rs, WS_FLAG + xc * 1024 + lane * 16);
            if (__all(f[0] == tag0 && f[1] == tag1 && f[2] == tag0 && f[3] == tag1)) break; }
    }
    bar();
    STAMP(4);
    if (wave < 4) for (int r = wave; r < 32; r += 4) glds(ws + WS_QA + (bb * 32 + r) * 1024 + ((lane ^ sw(r)) << 4), AUX + r * 1024, 2);
    bar();
    if (wave < 4) vm0(); else for (int r = NAr + NBr + wave - 4; r < NAr + NBr + NC_ROWS && r < 128; r += 4) dma_kv(r);
    bar();
    if (wave >= 4) for (int r = NAr + NBr + NC_ROWS + wave - 4; r < 128; r += 4) dma_kv(r);
    STAMP(5);
    constexpr unsigned WOI = 32768, PIM = 69632, SCR = 135168, WDI_ = 92160;
    const int u0_ = (9 * a) >> 1, nu_ = ((9 * (a + 1)) >> 1) - u0_;
    auto dma_gu = [&](int n) { int q = n * 64 + lane; int row = q / 10, k = q - row * 10; int ku = k >= 5 ? k - 5 : k; if (ku >= nu_) ku = nu_ - 1;
        glds((k >= 5 ? wu : wg) + (size_t)row * FF + (u0_ + ku) * 8, n * 1024, FFN_AUX); };
    auto dma_cw = [&](int n) { if (n < 32) { int e = 4 * n + g; int c = 32 * (e & 3) + 16 * ((e >> 2) & 1) + (e >> 3); glds(wuv + ((size_t)(128 * rr + c) * 32 + hh) * 128 + ((l15 ^ sw(e)) << 3), n * 1024, 1); }
        else glds(wo + (size_t)(hh * 128 + 32 * rr) * MD + (n - 32) * 512 + lane * 8, WOI + (n - 32) * 1024, 1); };
    {
        LAS float* mx = (LAS float*)(L + AUX);
        LAS float* lsum = (LAS float*)(L + AUX + 512);
        LAS char* pim = L + AUX + 1024;
        if (wave < 4) {
            bf16x8 qf[32], kf[16];
#pragma unroll
            for (int s = 0; s < 32; ++s) qf[s] = *(LAS bf16x8*)(L + a8[s & 7] + (s >> 3) * 256);
#pragma unroll
            for (int j = 0; j < 8; ++j) { a8[j] += (unsigned)(wave * 32768) - AUX; pin(a8[j]); }
            unsigned pw = AUX + 1024 + l31 * 272 + (32 * wave + 4 * hf) * 2; pin(pw);
            vm0();
            bar();
            STAMP(6);
            f32x16 s;
#pragma unroll
            for (int i = 0; i < 16; ++i) s[i] = 0.f;
#pragma unroll
            for (int st = 0; st < 16; ++st) kf[st] = *(LAS bf16x8*)(L + a8[st & 7] + (st >> 3) * 256);
            SB();
#pragma unroll
            for (int st = 0; st < 16; ++st) { s = mfma32(kf[st], qf[st], s); SB(); kf[st] = *(LAS bf16x8*)(L + a8[st & 7] + (2 + (st >> 3)) * 256); SB(); }
#pragma unroll
            for (int st = 0; st < 16; ++st) s = mfma32(kf[st], qf[16 + st], s);
            float m = s[0];
#pragma unroll
            for (int i = 1; i < 16; ++i) m = fmaxf(m, s[i]);
            m = max32(m);
            if (lane < 32) mx[wave * 32 + lane] = m;
            bar();
            const float M = fmaxf(fmaxf(mx[l31], mx[32 + l31]), fmaxf(mx[64 + l31], mx[96 + l31]));
            float ls = 0.f;
#pragma unroll
            for (int gq = 0; gq < 4; ++gq) { float e0 = ex2(s[4 * gq] - M), e1 = ex2(s[4 * gq + 1] - M), e2 = ex2(s[4 * gq + 2] - M), e3 = ex2(s[4 * gq + 3] - M);
                *(LAS v2u*)(L + pw + 16 * gq) = v2u{pk2(e0, e1), pk2(e2, e3)}; ls += (e0 + e1) + (e2 + e3); }
            ls = sum32(ls);
            if (lane < 32) lsum[wave * 32 + lane] = ls;
            bar();
            STAMP(7);
            bar();
        } else {
            const int cq = wave - 4, blk = g & 1;
            unsigned va[4], vb[4];
#pragma unroll
            for (int t = 0; t < 4; ++t) { const int ra = 8 * hf + q4, rb = ra + 4; int ch = 16 * cq + 4 * t + 2 * blk + (p4 >> 1);
                va[t] = ra * 1024 + ((ch ^ sw(ra)) << 4) + 8 * (p4 & 1); vb[t] = rb * 1024 + ((ch ^ sw(rb)) << 4) + 8 * (p4 & 1); pin(va[t]); pin(vb[t]); }
            unsigned pr = AUX + 1024 + l31 * 272 + 16 * hf, po = WS_PO + ((((bb * 32 + 4 * hf + blk) * 4 + cq) * 128 + split) * 16 + l15) * 16; pin(pr); pin(po);
            vm0();
            bar();
            bf16x8 vf[8][4];
#pragma unroll
            for (int ks = 0; ks < 4; ++ks)
#pragma unroll
                for (int t = 0; t < 4; ++t) vf[ks][t] = trfrag(L, va[t] + ks * 16384, vb[t] + ks * 16384);
#pragma unroll
            for (int t = 0; t < 4; ++t) { va[t] += 65536; vb[t] += 65536; }
#pragma unroll
            for (int ks = 0; ks < 4; ++ks)
#pragma unroll
                for (int t = 0; t < 4; ++t) vf[4 + ks][t] = trfrag(L, va[t] + ks * 16384, vb[t] + ks * 16384);
            bar();
            if (isQ) { for (int n = cq; n < NF_EARLY; n += 4) dma_gu(n); } else { for (int n = cq; n < NW_EARLY; n += 4) dma_cw(n); }
            bar();
            f32x16 o[4];
#pragma unroll
            for (int t = 0; t < 4; ++t)
#pragma unroll
                for (int i = 0; i < 16; ++i) o[t][i] = 0.f;
            bf16x8 pa[8];
#pragma unroll
            for (int ks = 0; ks < 8; ++ks) pa[ks] = *(LAS bf16x8*)(L + pr + 32 * ks);
#pragma unroll
            for (int ks = 0; ks < 8; ++ks)
#pragma unroll
                for (int t = 0; t < 4; ++t) o[t] = mfma32(pa[ks], vf[ks][t], o[t]);
#pragma unroll
            for (int r = 0; r < 16; r += 2) {
                auto x0 = __builtin_amdgcn_permlane16_swap(pk2(o[0][r], o[1][r]), pk2(o[0][r + 1], o[1][r + 1]), false, false);
                auto x1 = __builtin_amdgcn_permlane16_swap(pk2(o[2][r], o[3][r]), pk2(o[2][r + 1], o[3][r + 1]), false, false);
                st16(rs, po + ((r & 3) + 8 * (r >> 2)) * 131072, v4u{x0[0], x1[0], x0[1], x1[1]}); }
            v2u sv = {nosent(fmaxf(fmaxf(mx[l31], mx[32 + l31]), fmaxf(mx[64 + l31], mx[96 + l31]))), nosent((lsum[l31] + lsum[32 + l31]) + (lsum[64 + l31] + lsum[96 + l31]))};
            unsigned so = WS_ST + (((bb * 32 + l31) * 4 + cq) * 128 + split) * 8; pin(sv); pin(so);
            vm0();
            if (lane < 32) st8(rs, so, sv);
            bar();
        }
        STAMP(9);
    }
    if (isQ == (SWAP != 0)) {
        for (int n = NW_EARLY + wave; n < 68; n += 8) dma_cw(n);
        vm0();
        bar();
        bf16x8 fuv[4], fo[5];
#pragma unroll
        for (int ks = 0; ks < 4; ++ks) { fuv[ks] = trfrag(L, t0a + ks * 8192, t0b + ks * 8192); pin(fuv[ks]); }
#pragma unroll
        for (int i = 0; i < 5; ++i) { int n = wave + 8 * i; if (n > 35) n = 35; int ra = 8 * g + q4;
            fo[i] = trfrag(L, WOI + ra * 1152 + n * 32 + 8 * p4, WOI + (ra + 4) * 1152 + n * 32 + 8 * p4); pin(fo[i]); }
        STAMP(10);
        constexpr unsigned WIMG = SCR, LINV = SCR + 1280, AIMG = SCR + 1536, AIMG2 = SCR + 2816, R3 = SCR + 3328;
        unsigned pa_ = PIM + t0a, pb_ = PIM + t0b, wr = WIMG + b1 * 272 + 16 * g, aw = AIMG + b1 * 272 + (16 * wave + 4 * g) * 2, ar = AIMG + b1 * 272 + 16 * g;
        unsigned e4w = WS_E4 + ((((hh * 4 + (wave >> 1)) * 4 + rr) * 2 + b1) * 32 + 16 * (wave & 1) + 4 * g) * 4, e4r = WS_E4 + (hh * 4 + rr) * 1024 + lane * 16;
        unsigned a2w = AIMG2 + (l15 >> 3) * 80 + (lane & 7) * 8, a2r = AIMG2 + b1 * 80 + g * 16;
        unsigned e5w = WS_E5 + (((b1 * 18 + (wave >> 1)) * 128 + a) * 32 + 16 * (wave & 1) + 4 * g) * 4;
        pin(pa_); pin(pb_); pin(wr); pin(aw); pin(ar); pin(e4w); pin(e4r); pin(a2w); pin(a2r); pin(e5w);
        v2u sv[2];
#pragma unroll
        for (int h2 = 0; h2 < 2; ++h2) {
            const char* src[4];
#pragma unroll
            for (int k = 0; k < 4; ++k) { int n = wave + 8 * k; int b = n >> 4, row = 64 * h2 + 4 * (n & 15) + g; src[k] = ws + WS_PO + ((b * 32 + hh) * 4 + rr) * 32768 + row * 256 + ((l15 ^ sw(row)) << 4); pin(src[k]); }
            if (wave < 2) {
                for (int sp = 0; sp < SPIN_CAP; ++sp) { asm volatile("" ::: "memory"); sv[h2] = ld8(rs, WS_ST + (((wave * 32 + hh) * 4 + rr) * 128 + 64 * h2 + lane) * 8); if (__all(sv[h2][0] != SENT && sv[h2][1] != SENT)) break; }
            }
            bar();
#pragma unroll
            for (int k = 0; k < 4; ++k) { int n = wave + 8 * k; glds(src[k], PIM + (n >> 4) * 32768 + (16 * h2 + (n & 15)) * 1024, 2); }
        }
        STAMP(11);
        if (wave < 2) { float m0 = __uint_as_float(sv[0][0]), m1 = __uint_as_float(sv[1][0]);
            float M = max32(max16(rowmax(fmaxf(m0, m1))));
            unsigned wp = pk2(ex2(m0 - M), ex2(m1 - M));
            float Ls = sum32(halfsum(lo_f(wp) * __uint_as_float(sv[0][1]) + hi_f(wp) * __uint_as_float(sv[1][1])));
            *(LAS u16*)(L + WIMG + wave * 272 + lane * 2) = (u16)wp; *(LAS u16*)(L + WIMG + wave * 272 + 128 + lane * 2) = (u16)(wp >> 16);
            if (lane == 0) *(LAS float*)(L + LINV + wave * 4) = __builtin_amdgcn_rcpf(Ls); }
        vm0();
        bar();
        { f32x4 d0 = z4, d1 = z4;
#pragma unroll
          for (int ks = 0; ks < 4; ++ks) { bf16x8 bw = *(LAS bf16x8*)(L + wr + 64 * ks);
              d0 = mfma16(trfrag(L, pa_ + ks * 8192, pb_ + ks * 8192), bw, d0); d1 = mfma16(trfrag(L, pa_ + 32768 + ks * 8192, pb_ + 32768 + ks * 8192), bw, d1); }
          const float inv = *(LAS float*)(L + LINV + b1 * 4);
          f32x4 r = (b1 ? d1 : d0) * inv;
          if (l15 < 2) *(LAS v2u*)(L + aw) = v2u{pk2(r[0], r[1]), pk2(r[2], r[3])}; }
        bar();
        { f32x4 d = z4;
#pragma unroll
          for (int ks = 0; ks < 4; ++ks) d = mfma16(fuv[ks], *(LAS bf16x8*)(L + ar + 64 * ks), d);
          if (l15 < 2) st16(rs, e4w, nos4(d)); }
        STAMP(12);
        if (wave == 0) { v4u v;
            for (int sp = 0; sp < SPIN_CAP; ++sp) { asm volatile("" ::: "memory"); v = ld16(rs, e4r); if (__all(ok4(v))) break; }
            float c[4];
#pragma unroll
            for (int j = 0; j < 4; ++j) c[j] = sum32(sum16(__uint_as_float(v[j])));
            if (lane < 16) *(LAS v2u*)(L + a2w) = v2u{pk2(c[0], c[1]), pk2(c[2], c[3])}; }
        bar();
        { bf16x8 av = *(LAS bf16x8*)(L + a2r);
          f32x4 d[5];
#pragma unroll
          for (int i = 0; i < 5; ++i) d[i] = mfma16(fo[i], av, z4);
          if (l15 < 2) {
#pragma unroll
              for (int i = 0; i < 4; ++i) st16(rs, e5w + i * 65536, nos4(d[i]));
              if (wave < 4) st16(rs, e5w + 4 * 65536, nos4(d[4])); } }
        STAMP(13);
        const int rho = xc + 8 * idx;
        if (rho < 36) {
            LAS float* r3 = (LAS float*)(L + R3);
            f32x4 hv;
            const v2u xv = *(const v2u*)(x + rho * 32 + (lane & 7) * 4);
#pragma unroll
            for (int ph = 0; ph < 2; ++ph) {
                const unsigned src = (ph ? WS_E7 : WS_E5) + rho * 16384 + tid * 16;
                v4u v0, v1;
                for (int sp = 0; sp < SPIN_CAP; ++sp) { asm volatile("" ::: "memory"); v0 = ld16(rs, src); v1 = ld16(rs, src + 8192); if (__all(ok4(v0) && ok4(v1))) break; }
                f32x4 s;
#pragma unroll
                for (int j = 0; j < 4; ++j) { float t = __uint_as_float(v0[j]) + __uint_as_float(v1[j]); t += dpp<0x128>(t); s[j] = sum32(sum16(t)); }
                if (lane < 8) *(LAS f32x4*)(r3 + wave * 32 + lane * 4) = s;
                bar();
                if (wave == 0) { const int q = lane & 7; f32x4 t = z4;
#pragma unroll
                    for (int w = 0; w < 8; ++w) t += *(LAS f32x4*)(r3 + w * 32 + q * 4);
                    if (ph == 0) {
                        hv = t + f32x4{lo_f(xv[0]), hi_f(xv[0]), lo_f(xv[1]), hi_f(xv[1])};
                        unsigned h0 = pk2(hv[0], hv[1]), h1 = pk2(hv[2], hv[3]);
                        st8(rs, WS_H + (lane >> 3) * 8192 + rho * 64 + q * 8, v2u{__builtin_elementwise_min(h0, 0xFFFFFFFEu), __builtin_elementwise_min(h1, 0xFFFFFFFEu)});
                        f32x4 r = {lo_f(h0), hi_f(h0), lo_f(h1), hi_f(h1)};
                        float sm = (r[0] + r[1]) + (r[2] + r[3]); sm += dpp<0xB1>(sm); sm += dpp<0x4E>(sm); sm += dpp<0x141>(sm);
                        r -= sm * (1.f / 32.f);
                        float m2 = (r[0] * r[0] + r[1] * r[1]) + (r[2] * r[2] + r[3] * r[3]); m2 += dpp<0xB1>(m2); m2 += dpp<0x4E>(m2); m2 += dpp<0x141>(m2);
                        if (q == 0) st8(rs, WS_H + (lane >> 3) * 8192 + 4096 + rho * 8, v2u{nosent(sm), nosent(m2)});
                        if (rho == 0 && lane == 0) st8(rs, WS_EPOCH, v2u{tag0, tag1});
                    } else if (lane < 8) { *(v2u*)(out + rho * 32 + q * 4) = v2u{pk2(hv[0] + t[0], hv[1] + t[1]), pk2(hv[2] + t[2], hv[3] + t[3])}; } }
                bar();
                STAMP(14 + ph);
            }
        }
    } else {
        const int u0 = (9 * a) >> 1, nu = ((9 * (a + 1)) >> 1) - u0;
        constexpr unsigned WDI = 92160;
        if (FFN_SLEEP) __builtin_amdgcn_s_sleep(FFN_SLEEP);
        {
            for (int n = NF_EARLY + wave; n < 90; n += 8) dma_gu(n);
            for (int n = wave; n < 9 * nu; n += 8) glds(wd + (size_t)u0 * 8 * MD + n * 512 + lane * 8, WDI + n * 1024, 1);
        }
        vm0();
        bar();
        bf16x8 fg[3][5], fd[5][2];
#pragma unroll
        for (int kk = 0; kk < 3; ++kk) { int ks = wave + 8 * kk; if (ks > 17) ks = 17; int ra = 32 * ks + 8 * g + q4;
#pragma unroll
            for (int t = 0; t < 5; ++t) { fg[kk][t] = trfrag(L, ra * 160 + t * 32 + 8 * p4, (ra + 4) * 160 + t * 32 + 8 * p4); pin(fg[kk][t]); } }
#pragma unroll
        for (int i = 0; i < 5; ++i) { int n = wave + 8 * i; if (n > 35) n = 35; int ra = 8 * g + q4;
            fd[i][0] = trfrag(L, WDI + ra * 1152 + n * 32 + 8 * p4, WDI + (ra + 4) * 1152 + n * 32 + 8 * p4);
            int rc = 32 + q4;
            bf16x8 f1 = trfrag(L, WDI + rc * 1152 + n * 32 + 8 * p4, WDI + (rc + 4) * 1152 + n * 32 + 8 * p4);
            if (g != 0 || nu < 5) f1 = bf16x8{0, 0, 0, 0, 0, 0, 0, 0};
            fd[i][1] = f1; pin(fd[i][0]); pin(fd[i][1]); }
        constexpr unsigned RED2 = 144384, AIMG = 149504, STS = 150528;
        unsigned hsrc = WS_H + xc * 8192 + b1 * 1152 + (32 * wave + 8 * g) * 2, ssrc = WS_H + xc * 8192 + 4096 + ((wave & 1) * 18 + (l31 < 18 ? l31 : 0)) * 8;
        unsigned r2w = RED2 + ((wave * 2 + b1) * 80 + 4 * g) * 4, r2r = RED2 + ((wave & 1) * 80 + (lane < 40 ? lane : 0)) * 4, acw = AIMG + (wave & 1) * 144 + lane * 2, acr = AIMG + b1 * 144 + g * 16;
        unsigned e7w = WS_E7 + (((b1 * 18 + (wave >> 1)) * 128 + a) * 32 + 16 * (wave & 1) + 4 * g) * 4;
        float amask = lane < 8 * nu ? 1.f : 0.f, ok18 = l31 < 18 ? 1.f : 0.f;
        float csg = 0.f, csu = 0.f;
        {
            const bf16x8 ones = asb(v4u{0x3F803F80u, 0x3F803F80u, 0x3F803F80u, 0x3F803F80u});
            f32x4 d[5];
#pragma unroll
            for (int t = 0; t < 5; ++t) { d[t] = mfma16(fg[1][t], ones, mfma16(fg[0][t], ones, z4)); if (wave < 2) d[t] = mfma16(fg[2][t], ones, d[t]); }
            if (l15 < 2) {
#pragma unroll
                for (int t = 0; t < 5; ++t) *(LAS f32x4*)(L + r2w + 64 * t) = d[t]; }
            bar();
#pragma unroll
            for (int w = 0; w < 8; ++w) { csg += *(LAS float*)(L + r2r + w * 640); csu += *(LAS float*)(L + r2r + w * 640 + 160); }
            bar();
        }
        pin(hsrc); pin(ssrc); pin(r2w); pin(r2r); pin(acw); pin(acr); pin(e7w); pin(amask); pin(ok18); pin(csg); pin(csu);
        STAMP(10);
        { v4u hv[3]; v2u sv;
          for (int sp = 0; sp < SPIN_CAP; ++sp) { asm volatile("" ::: "memory");
              hv[0] = ld16(rs, hsrc); hv[1] = ld16(rs, hsrc + 512); bool ok = ok4(hv[0]) && ok4(hv[1]);
              if (wave < 2) { hv[2] = ld16(rs, hsrc + 1024); ok = ok && ok4(hv[2]); }
              if (wave >= 6) { sv = ld8(rs, ssrc); ok = ok && sv[0] != SENT && sv[1] != SENT; }
              if (__all(ok)) break; __builtin_amdgcn_s_sleep(2); }
          STAMP(11);
          f32x4 d[5];
#pragma unroll
          for (int t = 0; t < 5; ++t) d[t] = mfma16(fg[0][t], asb(hv[0]), z4);
#pragma unroll
          for (int t = 0; t < 5; ++t) d[t] = mfma16(fg[1][t], asb(hv[1]), d[t]);
          if (wave < 2) {
#pragma unroll
              for (int t = 0; t < 5; ++t) d[t] = mfma16(fg[2][t], asb(hv[2]), d[t]); }
          if (wave >= 6) { float sm = __uint_as_float(sv[0]) * ok18; float mu = halfsum(sm) * (1.f / 576.f); float dm = sm * (1.f / 32.f) - mu;
              float var = halfsum((__uint_as_float(sv[1]) + 32.f * dm * dm) * ok18) * (1.f / 576.f);
              if (lane == 0) *(LAS v2u*)(L + STS + (wave & 1) * 8) = v2u{__float_as_uint(mu), __float_as_uint(__builtin_amdgcn_rsqf(var + 1e-5f))}; }
          if (l15 < 2) {
#pragma unroll
              for (int t = 0; t < 5; ++t) *(LAS f32x4*)(L + r2w + 64 * t) = d[t]; } }
        bar();
        if (wave < 2) {
            float gs = 0.f, us = 0.f;
            const v2u st = *(LAS v2u*)(L + STS + wave * 8); const float mu = __uint_as_float(st[0]), rsd = __uint_as_float(st[1]);
#pragma unroll
            for (int w = 0; w < 8; ++w) { gs += *(LAS float*)(L + r2r + w * 640); us += *(LAS float*)(L + r2r + w * 640 + 160); }
            gs = (gs - mu * csg) * rsd; us = (us - mu * csu) * rsd;
            float act = gs * __builtin_amdgcn_rcpf(1.f + __expf(-gs)) * us;
            act = amask != 0.f ? act : 0.f;
            *(LAS u16*)(L + acw) = (u16)pk2(act, 0.f); }
        bar();
        { bf16x8 a0 = *(LAS bf16x8*)(L + acr), a1 = *(LAS bf16x8*)(L + acr + 64);
          f32x4 d[5];
#pragma unroll
          for (int i = 0; i < 5; ++i) d[i] = mfma16(fd[i][0], a0, z4);
#pragma unroll
          for (int i = 0; i < 5; ++i) d[i] = mfma16(fd[i][1], a1, d[i]);
          if (l15 < 2) {
#pragma unroll
              for (int i = 0; i < 4; ++i) st16(rs, e7w + i * 65536, nos4(d[i]));
              if (wave < 4) st16(rs, e7w + 4 * 65536, nos4(d[4])); } }
        STAMP(12);
    }
}

extern "C" void kernel_launch(void* const* d_in, const int* in_sizes, int n_in, void* d_out, int out_size, void* d_ws, size_t ws_size, hipStream_t stream) {
    (void)hipFuncSetAttribute((const void*)mk, hipFuncAttributeMaxDynamicSharedMemorySize, LDS_BYTES);
    mk<<<256, NT, LDS_BYTES, stream>>>((const u16*)d_in[0], (const u16*)d_in[1], (const u16*)d_in[2], (const u16*)d_in[3], (const u16*)d_in[4], (const u16*)d_in[5],
                                       (const u16*)d_in[7], (const u16*)d_in[8], (const u16*)d_in[9], (u16*)d_out, (char*)d_ws);
}
```

```cpp
#include <hip/hip_runtime.h>
#include <hip/hip_bf16.h>
#include <stdint.h>

typedef unsigned short u16;
typedef unsigned long long u64;
typedef short bf16x8 __attribute__((ext_vector_type(8)));
typedef short s16x4 __attribute__((ext_vector_type(4)));
typedef float f32x4 __attribute__((ext_vector_type(4)));
typedef float f32x16 __attribute__((ext_vector_type(16)));
typedef unsigned v4u __attribute__((ext_vector_type(4)));
typedef unsigned v2u __attribute__((ext_vector_type(2)));
#define LAS __attribute__((address_space(3)))
#define GAS __attribute__((address_space(1)))
#define DI __device__ __forceinline__

#define NA_ROWS 0
#define NB_ROWS 32

constexpr int SK = 16384, MD = 576, DC = 512, FF = 4608;
constexpr int NT = 512;
constexpr int LDS_BYTES = 163840;
constexpr int AUX = 131072;
constexpr unsigned WS_EPOCH = 0;
constexpr unsigned WS_FLAG = 4096;
constexpr unsigned WS_E1 = 32768;
constexpr unsigned WS_QA = WS_E1 + 262144;
constexpr unsigned WS_CLR = WS_QA + 65536;
constexpr unsigned WS_ST = WS_CLR;
constexpr unsigned WS_E4 = WS_ST + 262144;
constexpr unsigned WS_E5 = WS_E4 + 131072;
constexpr unsigned WS_E7 = WS_E5 + 589824;
constexpr unsigned WS_H = WS_E7 + 589824;
constexpr unsigned WS_CLR_END = WS_H + 65536 + 65536;
constexpr unsigned CLR_PER = (WS_CLR_END - WS_CLR) / 128;
static_assert(CLR_PER * 128 == WS_CLR_END - WS_CLR && CLR_PER % 1024 == 0, "clr");
constexpr unsigned WS_PO = 4194304;
constexpr unsigned WS_DBG = WS_PO + 8388608;
constexpr unsigned SENT = 0xFFFFFFFFu;
constexpr int SPIN_CAP = 400000;

DI float bf2f(u16 v) { return __uint_as_float((unsigned)v << 16); }
DI unsigned pk2(float a, float b) {
    typedef __bf16 bf2 __attribute__((ext_vector_type(2)));
    typedef float f2 __attribute__((ext_vector_type(2)));
    f2 v = {a, b}; bf2 r = __builtin_convertvector(v, bf2); return __builtin_bit_cast(unsigned, r);
}
DI float lo_f(unsigned p) { return __uint_as_float(p << 16); }
DI float hi_f(unsigned p) { return __uint_as_float(p & 0xffff0000u); }
DI unsigned nosent(float v) { return __builtin_elementwise_min(__float_as_uint(v), 0xFFFFFFFEu); }
template <class T> DI void pin(T& v) { asm volatile("" : "+v"(v)); }
DI float ex2(float v) { return __builtin_amdgcn_exp2f(v); }
#define NC_ROWS 32
#define E1_SLEEP 2
#define TOUCH 1
#define G1_EARLYW 1
#define FFN_SLEEP 0
#define FFN_AUX 0
#define SWAP 0
#define NF_EARLY 16
#define NW_EARLY 0
#define NODRAIN 0
#define ONEPHASE 1
#define H_SLEEP 2
#define R_SLEEP 0
#define WARM 0
#define VPRE 4
#define FFN_MAP 0
#define NA1 16
#define NA2 40
#define NB1 48
#define NB2 40
#define G1_SLEEP 0
#define G1_N0 1
#define G1_N1 4
#define G1_F1 1
#define G1_F2 5
DI int sw(int row) { return ((row & 3) << 2) | ((row >> 2) & 3); }
DI void bar() { asm volatile("s_waitcnt lgkmcnt(0)\n\ts_barrier" ::: "memory"); }
DI void glds(const void* g, unsigned lds_, int mode) {
    const unsigned lds = __builtin_amdgcn_readfirstlane(lds_);
    unsigned keep;
    if (mode == 1) asm volatile("s_mov_b32 %0, m0\n\ts_mov_b32 m0, %2\n\ts_nop 0\n\tglobal_load_lds_dwordx4 %1, off nt\n\ts_mov_b32 m0, %0" : "=&s"(keep) : "v"(g), "s"(lds) : "memory");
    else if (mode == 2) asm volatile("s_mov_b32 %0, m0\n\ts_mov_b32 m0, %2\n\ts_nop 0\n\tglobal_load_lds_dwordx4 %1, off sc1\n\ts_mov_b32 m0, %0" : "=&s"(keep) : "v"(g), "s"(lds) : "memory");
    else asm volatile("s_mov_b32 %0, m0\n\ts_mov_b32 m0, %2\n\ts_nop 0\n\tglobal_load_lds_dwordx4 %1, off\n\ts_mov_b32 m0, %0" : "=&s"(keep) : "v"(g), "s"(lds) : "memory");
}
DI void vm0() { asm volatile("s_waitcnt vmcnt(0)" ::: "memory"); }
DI v4u ld16(__amdgpu_buffer_rsrc_t rs, unsigned off) { return __builtin_amdgcn_raw_buffer_load_b128(rs, off, 0, 16); }
DI v2u ld8(__amdgpu_buffer_rsrc_t rs, unsigned off) { return __builtin_amdgcn_raw_buffer_load_b64(rs, off, 0, 16); }
DI void st16(__amdgpu_buffer_rsrc_t rs, unsigned off, v4u v) { __builtin_amdgcn_raw_buffer_store_b128(v, rs, off, 0, 16); }
DI void st8(__amdgpu_buffer_rsrc_t rs, unsigned off, v2u v) { __builtin_amdgcn_raw_buffer_store_b64(v, rs, off, 0, 16); }
DI bool ok4(v4u v) { return v[0] != SENT && v[1] != SENT && v[2] != SENT && v[3] != SENT; }
DI f32x4 mfma16(bf16x8 a, bf16x8 b, f32x4 c) { return __builtin_amdgcn_mfma_f32_16x16x32_bf16(a, b, c, 0, 0, 0); }
template <int P> DI float dpp(float v) { return __int_as_float(__builtin_amdgcn_mov_dpp(__float_as_int(v), P, 0xf, 0xf, true)); }
DI float sum16(float v) { auto r = __builtin_amdgcn_permlane16_swap(__float_as_uint(v), __float_as_uint(v), false, false); return __uint_as_float(r[0]) + __uint_as_float(r[1]); }
DI float sum32(float v) { auto r = __builtin_amdgcn_permlane32_swap(__float_as_uint(v), __float_as_uint(v), false, false); return __uint_as_float(r[0]) + __uint_as_float(r[1]); }
DI float max16(float v) { auto r = __builtin_amdgcn_permlane16_swap(__float_as_uint(v), __float_as_uint(v), false, false); return fmaxf(__uint_as_float(r[0]), __uint_as_float(r[1])); }
DI float max32(float v) { auto r = __builtin_amdgcn_permlane32_swap(__float_as_uint(v), __float_as_uint(v), false, false); return fmaxf(__uint_as_float(r[0]), __uint_as_float(r[1])); }
DI float rowsum(float v) { v += dpp<0x128>(v); v += dpp<0x124>(v); v += dpp<0x122>(v); v += dpp<0x121>(v); return v; }
DI float rowmax(float v) { v = fmaxf(v, dpp<0x128>(v)); v = fmaxf(v, dpp<0x124>(v)); v = fmaxf(v, dpp<0x122>(v)); v = fmaxf(v, dpp<0x121>(v)); return v; }
DI float halfsum(float v) { return sum16(rowsum(v)); }
DI float other_half(float v) { auto r = __builtin_amdgcn_permlane32_swap(__float_as_uint(v), __float_as_uint(v), false, false); return __uint_as_float((threadIdx.x & 32) ? r[0] : r[1]); }
DI f32x16 mfma32(bf16x8 a, bf16x8 b, f32x16 c) { return __builtin_amdgcn_mfma_f32_32x32x16_bf16(a, b, c, 0, 0, 0); }
DI bf16x8 trfrag(LAS char* L, unsigned a0, unsigned a1) {
    s16x4 x = __builtin_amdgcn_ds_read_tr16_b64_v4i16((LAS s16x4*)(L + a0));
    s16x4 y = __builtin_amdgcn_ds_read_tr16_b64_v4i16((LAS s16x4*)(L + a1));
    return __builtin_shufflevector(x, y, 0, 1, 2, 3, 4, 5, 6, 7);
}
DI bf16x8 asb(v4u v) { return __builtin_bit_cast(bf16x8, v); }
DI f32x4 fold(f32x4 d) { return f32x4{d[0] + dpp<0xEE>(d[0]), d[1] + dpp<0xEE>(d[1]), d[2] + dpp<0xEE>(d[2]), d[3] + dpp<0xEE>(d[3])}; }
DI v4u nos4(f32x4 d) { return v4u{nosent(d[0]), nosent(d[1]), nosent(d[2]), nosent(d[3])}; }
DI v4u split8(const float* n, bool lo_part) { v4u o;
#pragma unroll
    for (int j = 0; j < 4; ++j) { unsigned hi = pk2(n[2 * j], n[2 * j + 1]); o[j] = lo_part ? pk2(n[2 * j] - lo_f(hi), n[2 * j + 1] - hi_f(hi)) : hi; }
    return o; }
template <int N> DI void vmw() { asm volatile("s_waitcnt vmcnt(%0)" :: "n"(N) : "memory"); }

#define SB() __builtin_amdgcn_sched_barrier(0)

__global__ __launch_bounds__(NT, 2) void mk(const u16* __restrict__ x, const u16* __restrict__ ckv, const u16* __restrict__ wq, const u16* __restrict__ wuk,
                                           const u16* __restrict__ wuv, const u16* __restrict__ wo, const u16* __restrict__ wg, const u16* __restrict__ wu,
                                           const u16* __restrict__ wd, u16* __restrict__ out, char* __restrict__ ws) {
    extern __shared__ __attribute__((aligned(16))) char smem_[];
    LAS char* L = (LAS char*)smem_;
    const int tid = threadIdx.x, lane = tid & 63, wave = __builtin_amdgcn_readfirstlane(tid >> 6);
    const int g = lane >> 4, l15 = lane & 15, l31 = lane & 31, hf = lane >> 5;
    const int q4 = l15 >> 2, p4 = lane & 3, b1 = lane & 1;
    const int bid = blockIdx.x, xc = bid & 7, jj = bid >> 3;
    const bool isQ = jj < 16;
    const int idx = jj & 15, a = xc * 16 + idx;
    const int hh = xc * 4 + (idx >> 2), rr = idx & 3;
    const int bb = a >> 6, split = (a & 63) + (isQ ? 64 : 0);
    const __amdgpu_buffer_rsrc_t rs = __builtin_amdgcn_make_buffer_rsrc(ws, 0, 0x10000000, 0x00020000);
    const u16* kvg = ckv + ((size_t)bb * SK + split * 128) * DC;
    const f32x4 z4 = {0.f, 0.f, 0.f, 0.f};
    auto dma_kv = [&](int row) { glds(kvg + (size_t)row * DC + ((lane ^ sw(row)) << 3), (unsigned)row * 1024u, 1); };
    unsigned t0a, t0b;
    { int ra = 8 * g + q4, rb = ra + 4, ch = 2 * wave + (p4 >> 1); t0a = ra * 256 + ((ch ^ sw(ra)) << 4) + 8 * (p4 & 1); t0b = rb * 256 + ((ch ^ sw(rb)) << 4) + 8 * (p4 & 1); }
    unsigned tag0, tag1;
    bf16x8 fuv[4];
    constexpr unsigned XRAW = AUX + 20480, EPL = AUX + 24576, QIMG = AUX + 26624, WQI = 88 * 1024, WKI = 96 * 1024;
    if (!isQ) {
        const int nk = rr < 2 ? 5 : 4, row0 = rr < 2 ? 160 * rr : 64 + 128 * rr;
        v4u xf[5], xs[3]; v2u ep;
        if (wave == 7) {
            glds(x + lane * 8, XRAW, 0); glds(x + 512 + lane * 8, XRAW + 1024, 0); glds(x + 1024 + (lane & 15) * 8, XRAW + 2048, 0);
            glds(ws + WS_EPOCH + lane * 16, EPL, 2);
        }
        for (int n = wave; n < 8 * nk; n += 8) { int row = 4 * n + g; glds(wq + (size_t)(row0 + row) * 4096 + hh * 128 + ((l15 ^ sw(row)) << 3), WQI + n * 1024, 1); }
#pragma unroll
        for (int i = 0; i < NA1 / 8; ++i) dma_kv(wave + 8 * i);
        unsigned e1w = WS_E1 + ((hh * 4 + rr) * 2 + b1) * 1024 + (16 * wave + 4 * g + (l15 & 2)) * 8;
        unsigned wa = WQI + t0a, wb = WQI + t0b;
        pin(e1w); pin(wa); pin(wb);
        if (wave == 7) vmw<4 + NA1 / 8>();
        bar();
        ep = *(LAS v2u*)(L + EPL);
        xs[0] = *(LAS v4u*)(L + XRAW + hf * 1152 + l31 * 16); xs[1] = *(LAS v4u*)(L + XRAW + hf * 1152 + 512 + l31 * 16); xs[2] = *(LAS v4u*)(L + XRAW + hf * 1152 + 1024 + (l31 & 7) * 16);
#pragma unroll
        for (int ks = 0; ks < 5; ++ks) xf[ks] = *(LAS v4u*)(L + XRAW + b1 * 1152 + (row0 + 32 * (ks < nk ? ks : 0) + 8 * g) * 2);
        { u64 t = (((u64)ep[1] << 32) | ep[0]) + 1ull; tag0 = (unsigned)t; tag1 = (unsigned)(t >> 32); }
        float mu, rsd;
        {
            const float ok2 = l31 < 8 ? 1.f : 0.f;
            float v[24]; float s = 0.f;
#pragma unroll
            for (int i = 0; i < 3; ++i)
#pragma unroll
                for (int j = 0; j < 4; ++j) { v[8 * i + 2 * j] = lo_f(xs[i][j]); v[8 * i + 2 * j + 1] = hi_f(xs[i][j]); }
            float s2 = 0.f;
#pragma unroll
            for (int i = 0; i < 16; ++i) s += v[i];
#pragma unroll
            for (int i = 16; i < 24; ++i) s2 += v[i];
            mu = halfsum(s + ok2 * s2) * (1.f / 576.f); float q = 0.f, q2 = 0.f;
#pragma unroll
            for (int i = 0; i < 16; ++i) { float d = v[i] - mu; q += d * d; }
#pragma unroll
            for (int i = 16; i < 24; ++i) { float d = v[i] - mu; q2 += d * d; }
            rsd = __builtin_amdgcn_rsqf(halfsum(q + ok2 * q2) * (1.f / 576.f) + 1e-5f);
            float mo = other_half(mu), ro = other_half(rsd);
            if (b1 != hf) { mu = mo; rsd = ro; }
            if (l15 == 2) {
#pragma unroll
                for (int ks = 0; ks < 5; ++ks) xf[ks] = v4u{0x3F803F80u, 0x3F803F80u, 0x3F803F80u, 0x3F803F80u}; }
#pragma unroll
            for (int ks = 0; ks < 5; ++ks) pin(xf[ks]);
        }
        unsigned t40lo = tag0 << 24, t40hi = (tag0 >> 8) | (tag1 << 24);
        pin(t40lo); pin(t40hi); pin(mu); pin(rsd);
        vmw<NA1 / 8>();
        bar();
        { f32x4 d = z4;
#pragma unroll
          for (int ks = 0; ks < 4; ++ks) d = mfma16(trfrag(L, wa + ks * 8192, wb + ks * 8192), asb(xf[ks]), d);
          if (nk == 5) d = mfma16(trfrag(L, wa + 32768, wb + 32768), asb(xf[4]), d);
#pragma unroll
          for (int j = 0; j < 4; ++j) d[j] = (d[j] - mu * dpp<0xAA>(d[j])) * rsd;
          float e0 = dpp<0x44>(d[2]), e1 = dpp<0x44>(d[3]);
          if (l15 & 2) { d[0] = e0; d[1] = e1; }
          if (l15 < 4) st16(rs, e1w, v4u{((__float_as_uint(d[0]) + 0x80u) >> 8) | t40lo, t40hi, ((__float_as_uint(d[1]) + 0x80u) >> 8) | t40lo, t40hi}); }
        bar();
        for (int n = wave; n < 32; n += 8) { int e = 4 * n + g; int c = 32 * (e & 3) + 16 * ((e >> 2) & 1) + (e >> 3); glds(wuv + ((size_t)(128 * rr + c) * 32 + hh) * 128 + ((l15 ^ sw(e)) << 3), AUX + n * 1024, 1); }
#pragma unroll
        for (int i = NA1 / 8; i < 16; ++i) { dma_kv(wave + 8 * i); if (i >= G1_F2) vmw<G1_F2>(); }
        bar();
#pragma unroll
        for (int ks = 0; ks < 4; ++ks) { fuv[ks] = trfrag(L, AUX + t0a + ks * 8192 - 65536 + 65536, AUX + t0b + ks * 8192); pin(fuv[ks]); }
        vm0();
    } else {
        if (wave == 7) glds(ws + WS_EPOCH + lane * 16, EPL, 2);
        if (wave == 6) { const char* tp = lane < 8 ? ws + WS_FLAG + lane * 1024 + (a >> 4) * 128 : ws + WS_QA + (((lane & 1) * 32 + hh) * 512 + 128 * rr + ((lane >> 1) & 1) * 64) * 2; glds(tp, EPL + 3072, 2); }
        for (int n = wave; n < 32; n += 8) { int row = 4 * n + g; glds(wuk + ((size_t)(128 * rr + row) * 32 + hh) * 128 + ((l15 ^ sw(row)) << 3), WKI + n * 1024, 1); }
        { v4u s4 = {SENT, SENT, SENT, SENT};
          st16(rs, WS_CLR + a * CLR_PER + wave * 1024 + lane * 16, s4);
          st16(rs, WS_CLR + a * CLR_PER + 8192 + (wave < 5 ? wave : 0) * 1024 + lane * 16, s4); }
#pragma unroll
        for (int ks = 0; ks < 4; ++ks) fuv[ks] = bf16x8{0, 0, 0, 0, 0, 0, 0, 0};
#pragma unroll
        for (int i = 0; i < NA2 / 8; ++i) dma_kv(wave + 8 * i);
        unsigned e1r = WS_E1 + ((hh * 4 + g) * 2 + (l15 >> 3)) * 1024 + (16 * wave + 2 * (lane & 7)) * 8;
        unsigned qw = QIMG + (l15 >> 3) * 272 + (16 * wave + 2 * (lane & 7)) * 2;
        unsigned ka0, ka1, qr, qaw;
        { const int c0 = 32 * (wave & 3) + 8 * q4 + (l15 & 3), c1 = c0 + 4; ka0 = WKI + c0 * 256 + ((g ^ sw(c0)) << 4); ka1 = WKI + c1 * 256 + ((g ^ sw(c1)) << 4);
          qr = QIMG + b1 * 272 + 16 * g; qaw = WS_QA + ((b1 * 32 + hh) * 512 + 128 * rr + 32 * (wave & 3) + 8 * g) * 2; }
        pin(e1r); pin(qw); pin(ka0); pin(ka1); pin(qr); pin(qaw);
        vm0();
        bar();
        { v2u ep = *(LAS v2u*)(L + EPL); u64 t = (((u64)ep[1] << 32) | ep[0]) + 1ull; tag0 = (unsigned)t; tag1 = (unsigned)(t >> 32); }
        const unsigned t40lo = tag0 << 24, t40hi = (tag0 >> 8) | (tag1 << 24);
        { v4u v; bool okv = false;
          for (int sp = 0; sp < SPIN_CAP; ++sp) { asm volatile("" ::: "memory");
              if (!okv) v = ld16(rs, e1r);
              okv = (v[1] == t40hi) & (v[3] == t40hi) & ((v[0] & 0xFF000000u) == t40lo) & ((v[2] & 0xFF000000u) == t40lo);
              if (__all(okv)) break; if (E1_SLEEP) __builtin_amdgcn_s_sleep(E1_SLEEP); }
          float q0 = sum32(sum16(__uint_as_float(v[0] << 8))), q1 = sum32(sum16(__uint_as_float(v[2] << 8)));
          if (lane < 16) *(LAS unsigned*)(L + qw) = pk2(q0, q1);
        }
        bar();
        if (wave < 4) {
            f32x4 d0 = z4, d1 = z4;
#pragma unroll
            for (int ks = 0; ks < 4; ++ks) { bf16x8 bq = *(LAS bf16x8*)(L + qr + 64 * ks);
                d0 = mfma16(*(LAS bf16x8*)(L + (ka0 ^ (ks << 6))), bq, d0); d1 = mfma16(*(LAS bf16x8*)(L + (ka1 ^ (ks << 6))), bq, d1); }
            const float sc = 0.08838834764831845f * 1.4426950408889634f;
            d0 *= sc; d1 *= sc;
            if (l15 < 2) st16(rs, qaw, v4u{pk2(d0[0], d0[1]), pk2(d0[2], d0[3]), pk2(d1[0], d1[1]), pk2(d1[2], d1[3])});
            vm0();
        }
        bar();
        if (wave == 0 && lane < 8) st8(rs, WS_FLAG + lane * 1024 + a * 8, v2u{tag0, tag1});
        if (wave >= 4) for (int r = NA2 + wave - 4; r < NA2 + NB2; r += 4) dma_kv(r);
    }
    const int NAr = isQ ? NA2 : 128, NBr = isQ ? NB2 : 0;
    unsigned a8[8];
#pragma unroll
    for (int j = 0; j < 8; ++j) { a8[j] = AUX + l31 * 1024 + (((2 * j + hf) ^ sw(l31)) << 4); pin(a8[j]); }
    if (wave == 0) {
        for (int sp = 0; sp < SPIN_CAP; ++sp) { asm volatile("" ::: "memory"); v4u f = ld16(rs, WS_FLAG + xc * 1024 + lane * 16);
            if (__all(f[0] == tag0 && f[1] == tag1 && f[2] == tag0 && f[3] == tag1)) break; }
    }
    bar();
    if (wave < 4) for (int r = wave; r < 32; r += 4) glds(ws + WS_QA + (bb * 32 + r) * 1024 + ((lane ^ sw(r)) << 4), AUX + r * 1024, 2);
    bar();
    if (wave < 4) vm0(); else for (int r = NAr + NBr + wave - 4; r < NAr + NBr + NC_ROWS && r < 128; r += 4) dma_kv(r);
    bar();
    if (wave >= 4) for (int r = NAr + NBr + NC_ROWS + wave - 4; r < 128; r += 4) dma_kv(r);
    constexpr unsigned WOI = 32768, PIM = 69632, SCR = 135168, WDI_ = 92160;
    const int af_ = FFN_MAP ? idx * 8 + xc : a;
    const int u0_ = (9 * af_) >> 1, nu_ = ((9 * (af_ + 1)) >> 1) - u0_;
    auto dma_gu = [&](int n) { int q = n * 64 + lane; int row = q / 10, k = q - row * 10; int ku = k >= 5 ? k - 5 : k; if (ku >= nu_) ku = nu_ - 1;
        glds((k >= 5 ? wu : wg) + (size_t)row * FF + (u0_ + ku) * 8, n * 1024, FFN_AUX); };
    auto dma_cw = [&](int n) { glds(wo + (size_t)(hh * 128 + 32 * rr) * MD + n * 512 + lane * 8, WOI + n * 1024, 1); };
    {
        LAS float* mx = (LAS float*)(L + AUX);
        LAS float* lsum = (LAS float*)(L + AUX + 512);
        LAS char* pim = L + AUX + 1024;
        if (wave < 4) {
            bf16x8 qf[32], kf[16];
#pragma unroll
            for (int s = 0; s < 32; ++s) qf[s] = *(LAS bf16x8*)(L + a8[s & 7] + (s >> 3) * 256);
#pragma unroll
            for (int j = 0; j < 8; ++j) { a8[j] += (unsigned)(wave * 32768) - AUX; pin(a8[j]); }
            unsigned pw = AUX + 1024 + l31 * 272 + (32 * wave + 4 * hf) * 2; pin(pw);
            vm0();
            bar();
            f32x16 s;
#pragma unroll
            for (int i = 0; i < 16; ++i) s[i] = 0.f;
#pragma unroll
            for (int st = 0; st < 16; ++st) kf[st] = *(LAS bf16x8*)(L + a8[st & 7] + (st >> 3) * 256);
            SB();
#pragma unroll
            for (int st = 0; st < 16; ++st) { s = mfma32(kf[st], qf[st], s); SB(); kf[st] = *(LAS bf16x8*)(L + a8[st & 7] + (2 + (st >> 3)) * 256); SB(); }
#pragma unroll
            for (int st = 0; st < 16; ++st) s = mfma32(kf[st], qf[16 + st], s);
            float m = s[0];
#pragma unroll
            for (int i = 1; i < 16; ++i) m = fmaxf(m, s[i]);
            m = max32(m);
            if (lane < 32) mx[wave * 32 + lane] = m;
            bar();
            const float M = fmaxf(fmaxf(mx[l31], mx[32 + l31]), fmaxf(mx[64 + l31], mx[96 + l31]));
            float ls = 0.f;
#pragma unroll
            for (int gq = 0; gq < 4; ++gq) { float e0 = ex2(s[4 * gq] - M), e1 = ex2(s[4 * gq + 1] - M), e2 = ex2(s[4 * gq + 2] - M), e3 = ex2(s[4 * gq + 3] - M);
                *(LAS v2u*)(L + pw + 16 * gq) = v2u{pk2(e0, e1), pk2(e2, e3)}; ls += (e0 + e1) + (e2 + e3); }
            ls = sum32(ls);
            if (lane < 32) lsum[wave * 32 + lane] = ls;
            bar();
            if (isQ) { for (int n = wave; n < NF_EARLY; n += 4) dma_gu(n); } else { for (int n = wave; n < NW_EARLY; n += 4) dma_cw(n); }
            bar();
        } else {
            const int cq = wave - 4, blk = g & 1;
            unsigned va[4], vb[4];
#pragma unroll
            for (int t = 0; t < 4; ++t) { const int ra = 8 * hf + q4, rb = ra + 4; int ch = 16 * cq + 4 * t + 2 * blk + (p4 >> 1);
                va[t] = ra * 1024 + ((ch ^ sw(ra)) << 4) + 8 * (p4 & 1); vb[t] = rb * 1024 + ((ch ^ sw(rb)) << 4) + 8 * (p4 & 1); pin(va[t]); pin(vb[t]); }
            unsigned pr = AUX + 1024 + l31 * 272 + 16 * hf, po = WS_PO + ((((bb * 32 + 4 * hf + blk) * 4 + cq) * 128 + split) * 16 + l15) * 16; pin(pr); pin(po);
            vm0();
            bar();
            bf16x8 vf[8][4];
#pragma unroll
            for (int ks = 0; ks < 4; ++ks)
#pragma unroll
                for (int t = 0; t < 4; ++t) vf[ks][t] = trfrag(L, va[t] + ks * 16384, vb[t] + ks * 16384);
#pragma unroll
            for (int t = 0; t < 4; ++t) { va[t] += 65536; vb[t] += 65536; }
#pragma unroll
            for (int ks = 0; ks < VPRE; ++ks)
#pragma unroll
                for (int t = 0; t < 4; ++t) vf[4 + ks][t] = trfrag(L, va[t] + ks * 16384, vb[t] + ks * 16384);
            bar();
#pragma unroll
            for (int ks = VPRE; ks < 4; ++ks)
#pragma unroll
                for (int t = 0; t < 4; ++t) vf[4 + ks][t] = trfrag(L, va[t] + ks * 16384, vb[t] + ks * 16384);
            bar();
            f32x16 o[4];
#pragma unroll
            for (int t = 0; t < 4; ++t)
#pragma unroll
                for (int i = 0; i < 16; ++i) o[t][i] = 0.f;
            bf16x8 pa[8];
#pragma unroll
            for (int ks = 0; ks < 8; ++ks) pa[ks] = *(LAS bf16x8*)(L + pr + 32 * ks);
#pragma unroll
            for (int ks = 0; ks < 8; ++ks)
#pragma unroll
                for (int t = 0; t < 4; ++t) o[t] = mfma32(pa[ks], vf[ks][t], o[t]);
#pragma unroll
            for (int r = 0; r < 16; r += 2) {
                auto x0 = __builtin_amdgcn_permlane16_swap(pk2(o[0][r], o[1][r]), pk2(o[0][r + 1], o[1][r + 1]), false, false);
                auto x1 = __builtin_amdgcn_permlane16_swap(pk2(o[2][r], o[3][r]), pk2(o[2][r + 1], o[3][r + 1]), false, false);
                st16(rs, po + ((r & 3) + 8 * (r >> 2)) * 131072, v4u{x0[0], x1[0], x0[1], x1[1]}); }
            v2u sv = {nosent(fmaxf(fmaxf(mx[l31], mx[32 + l31]), fmaxf(mx[64 + l31], mx[96 + l31]))), nosent((lsum[l31] + lsum[32 + l31]) + (lsum[64 + l31] + lsum[96 + l31]))};
            unsigned so = WS_ST + (((bb * 32 + l31) * 4 + cq) * 128 + split) * 8; pin(sv); pin(so);
            if (!(NODRAIN && isQ)) vm0();
            if (lane < 32) st8(rs, so, sv);
            bar();
        }
    }
    if (isQ == (SWAP != 0)) {
        for (int n = NW_EARLY + wave; n < 36; n += 8) dma_cw(n);
        vm0();
        bar();
        bf16x8 fo[5];
#pragma unroll
        for (int i = 0; i < 5; ++i) { int n = wave + 8 * i; if (n > 35) n = 35; int ra = 8 * g + q4;
            fo[i] = trfrag(L, WOI + ra * 1152 + n * 32 + 8 * p4, WOI + (ra + 4) * 1152 + n * 32 + 8 * p4); pin(fo[i]); }
        constexpr unsigned WIMG = SCR, LINV = SCR + 1280, AIMG = SCR + 1536, AIMG2 = SCR + 2816, R3 = SCR + 3328;
        unsigned pa_ = PIM + t0a, pb_ = PIM + t0b, wr = WIMG + b1 * 272 + 16 * g, aw = AIMG + b1 * 272 + (16 * wave + 4 * g) * 2, ar = AIMG + b1 * 272 + 16 * g;
        unsigned e4w = WS_E4 + ((((hh * 4 + (wave >> 1)) * 4 + rr) * 2 + b1) * 32 + 16 * (wave & 1) + 4 * g) * 4, e4r = WS_E4 + (hh * 4 + rr) * 1024 + lane * 16;
        unsigned a2w = AIMG2 + (l15 >> 3) * 80 + (lane & 7) * 8, a2r = AIMG2 + b1 * 80 + g * 16;
        unsigned e5w = WS_E5 + (((b1 * 18 + (wave >> 1)) * 128 + a) * 32 + 16 * (wave & 1) + 4 * g) * 4;
        pin(pa_); pin(pb_); pin(wr); pin(aw); pin(ar); pin(e4w); pin(e4r); pin(a2w); pin(a2r); pin(e5w);
        v2u sv[2];
        const char* src[4];
        if (tid == 0) *(LAS unsigned*)(L + LINV + 16) = 0u;
        {
#pragma unroll
            for (int k = 0; k < 4; ++k) { int n = wave + 8 * k; int b = n >> 4, row = 64 + 4 * (n & 15) + g; src[k] = ws + WS_PO + ((b * 32 + hh) * 4 + rr) * 32768 + row * 256 + ((l15 ^ sw(row)) << 4); pin(src[k]); }
            if (wave < 2) { const unsigned so_ = WS_ST + (((wave * 32 + hh) * 4 + rr) * 128 + lane) * 8;
                for (int sp = 0; sp < SPIN_CAP; ++sp) { asm volatile("" ::: "memory"); sv[0] = ld8(rs, so_); sv[1] = ld8(rs, so_ + 512); if (__all(sv[0][0] != SENT && sv[0][1] != SENT && sv[1][0] != SENT && sv[1][1] != SENT)) break; }
            }
            bar();
#pragma unroll
            for (int k = 0; k < 4; ++k) { int n = wave + 8 * k; glds(src[k] - 16384, PIM + (n >> 4) * 32768 + (n & 15) * 1024, 2); }
#pragma unroll
            for (int k = 0; k < 4; ++k) { int n = wave + 8 * k; glds(src[k], PIM + (n >> 4) * 32768 + (16 + (n & 15)) * 1024, 2); }
        }
        if (wave < 2) { float m0 = __uint_as_float(sv[0][0]), m1 = __uint_as_float(sv[1][0]);
            float M = max32(max16(rowmax(fmaxf(m0, m1))));
            unsigned wp = pk2(ex2(m0 - M), ex2(m1 - M));
            float Ls = sum32(halfsum(lo_f(wp) * __uint_as_float(sv[0][1]) + hi_f(wp) * __uint_as_float(sv[1][1])));
            *(LAS u16*)(L + WIMG + wave * 272 + lane * 2) = (u16)wp; *(LAS u16*)(L + WIMG + wave * 272 + 128 + lane * 2) = (u16)(wp >> 16);
            if (lane == 0) *(LAS float*)(L + LINV + wave * 4) = __builtin_amdgcn_rcpf(Ls); }
        for (unsigned it = 1;; ++it) {
            vm0();
            bar();
            f32x4 d0 = z4, d1 = z4;
            typedef unsigned short us2 __attribute__((ext_vector_type(2)));
            us2 mxu = {0, 0};
#pragma unroll
            for (int ks = 0; ks < 4; ++ks) { bf16x8 bw = *(LAS bf16x8*)(L + wr + 64 * ks);
                bf16x8 f0 = trfrag(L, pa_ + ks * 8192, pb_ + ks * 8192), f1 = trfrag(L, pa_ + 32768 + ks * 8192, pb_ + 32768 + ks * 8192);
                d0 = mfma16(f0, bw, d0); d1 = mfma16(f1, bw, d1);
                if (NODRAIN && ks >= 2) { v4u u0 = __builtin_bit_cast(v4u, f0), u1 = __builtin_bit_cast(v4u, f1);
#pragma unroll
                    for (int j = 0; j < 4; ++j) { mxu = __builtin_elementwise_max(mxu, __builtin_bit_cast(us2, u0[j])); mxu = __builtin_elementwise_max(mxu, __builtin_bit_cast(us2, u1[j])); } } }
            const float inv = *(LAS float*)(L + LINV + b1 * 4);
            f32x4 r = (b1 ? d1 : d0) * inv;
            if (l15 < 2) *(LAS v2u*)(L + aw) = v2u{pk2(r[0], r[1]), pk2(r[2], r[3])};
            if (NODRAIN && (mxu[0] == 0xFFFFu || mxu[1] == 0xFFFFu)) *(LAS unsigned*)(L + LINV + 16) = it;
            bar();
            if (!NODRAIN) break;
            if (__builtin_amdgcn_readfirstlane(*(LAS unsigned*)(L + LINV + 16)) != it || it > 2000u) break;
#pragma unroll
            for (int k = 0; k < 4; ++k) { int n = wave + 8 * k; glds(src[k], PIM + (n >> 4) * 32768 + (16 + (n & 15)) * 1024, 2); }
        }
        { f32x4 d = z4;
#pragma unroll
          for (int ks = 0; ks < 4; ++ks) d = mfma16(fuv[ks], *(LAS bf16x8*)(L + ar + 64 * ks), d);
          if (l15 < 2) st16(rs, e4w, nos4(d)); }
        if (wave == 0) { v4u v;
            for (int sp = 0; sp < SPIN_CAP; ++sp) { asm volatile("" ::: "memory"); v = ld16(rs, e4r); if (__all(ok4(v))) break; }
            float c[4];
#pragma unroll
            for (int j = 0; j < 4; ++j) c[j] = sum32(sum16(__uint_as_float(v[j])));
            if (lane < 16) *(LAS v2u*)(L + a2w) = v2u{pk2(c[0], c[1]), pk2(c[2], c[3])}; }
        bar();
        { bf16x8 av = *(LAS bf16x8*)(L + a2r);
          f32x4 d[5];
#pragma unroll
          for (int i = 0; i < 5; ++i) d[i] = mfma16(fo[i], av, z4);
          if (l15 < 2) {
#pragma unroll
              for (int i = 0; i < 4; ++i) st16(rs, e5w + i * 65536, nos4(d[i]));
              if (wave < 4) st16(rs, e5w + 4 * 65536, nos4(d[4])); } }
        const int rho = xc + 8 * idx;
        if (rho < 36) {
            LAS float* r3 = (LAS float*)(L + R3);
            f32x4 hv;
            const v2u xv = *(const v2u*)(x + rho * 32 + (lane & 7) * 4);
#pragma unroll
            for (int ph = 0; ph < 2; ++ph) {
                const unsigned src = (ph ? WS_E7 : WS_E5) + rho * 16384 + tid * 16;
                v4u v0, v1;
                bool k0 = false, k1 = false;
                for (int sp = 0; sp < SPIN_CAP; ++sp) { asm volatile("" ::: "memory"); if (!k0) v0 = ld16(rs, src); if (!k1) v1 = ld16(rs, src + 8192); k0 = ok4(v0); k1 = ok4(v1); if (__all(k0 && k1)) break; if (R_SLEEP) __builtin_amdgcn_s_sleep(R_SLEEP); }
                f32x4 s;
#pragma unroll
                for (int j = 0; j < 4; ++j) { float t = __uint_as_float(v0[j]) + __uint_as_float(v1[j]); t += dpp<0x128>(t); s[j] = sum32(sum16(t)); }
                if (lane < 8) *(LAS f32x4*)(r3 + wave * 32 + lane * 4) = s;
                bar();
                if (wave == 0) { const int q = lane & 7; f32x4 t = z4;
#pragma unroll
                    for (int w = 0; w < 8; ++w) t += *(LAS f32x4*)(r3 + w * 32 + q * 4);
                    if (ph == 0) {
                        hv = t + f32x4{lo_f(xv[0]), hi_f(xv[0]), lo_f(xv[1]), hi_f(xv[1])};
                        unsigned h0 = pk2(hv[0], hv[1]), h1 = pk2(hv[2], hv[3]);
                        st8(rs, WS_H + (lane >> 3) * 8192 + rho * 64 + q * 8, v2u{__builtin_elementwise_min(h0, 0xFFFFFFFEu), __builtin_elementwise_min(h1, 0xFFFFFFFEu)});
                        f32x4 r = {lo_f(h0), hi_f(h0), lo_f(h1), hi_f(h1)};
                        float sm = (r[0] + r[1]) + (r[2] + r[3]); sm += dpp<0xB1>(sm); sm += dpp<0x4E>(sm); sm += dpp<0x141>(sm);
                        r -= sm * (1.f / 32.f);
                        float m2 = (r[0] * r[0] + r[1] * r[1]) + (r[2] * r[2] + r[3] * r[3]); m2 += dpp<0xB1>(m2); m2 += dpp<0x4E>(m2); m2 += dpp<0x141>(m2);
                        if (q == 0) st8(rs, WS_H + (lane >> 3) * 8192 + 4096 + rho * 8, v2u{nosent(sm), nosent(m2)});
                        if (rho == 0 && lane == 0) st8(rs, WS_EPOCH, v2u{tag0, tag1});
                    } else if (lane < 8) { *(v2u*)(out + rho * 32 + q * 4) = v2u{pk2(hv[0] + t[0], hv[1] + t[1]), pk2(hv[2] + t[2], hv[3] + t[3])}; } }
                bar();
            }
        }
    } else {
        const int u0 = u0_, nu = nu_;
        constexpr unsigned WDI = 92160;
        if (FFN_SLEEP) __builtin_amdgcn_s_sleep(FFN_SLEEP);
        {
            for (int n = NF_EARLY + wave; n < 90; n += 8) dma_gu(n);
            for (int n = wave; n < 9 * nu; n += 8) glds(wd + (size_t)u0 * 8 * MD + n * 512 + lane * 8, WDI + n * 1024, 1);
        }
        vm0();
        bar();
        bf16x8 fg[3][5], fd[5][2];
#pragma unroll
        for (int kk = 0; kk < 3; ++kk) { int ks = wave + 8 * kk; if (ks > 17) ks = 17; int ra = 32 * ks + 8 * g + q4;
#pragma unroll
            for (int t = 0; t < 5; ++t) { fg[kk][t] = trfrag(L, ra * 160 + t * 32 + 8 * p4, (ra + 4) * 160 + t * 32 + 8 * p4); pin(fg[kk][t]); } }
#pragma unroll
        for (int i = 0; i < 5; ++i) { int n = wave + 8 * i; if (n > 35) n = 35; int ra = 8 * g + q4;
            fd[i][0] = trfrag(L, WDI + ra * 1152 + n * 32 + 8 * p4, WDI + (ra + 4) * 1152 + n * 32 + 8 * p4);
            int rc = 32 + q4;
            bf16x8 f1 = trfrag(L, WDI + rc * 1152 + n * 32 + 8 * p4, WDI + (rc + 4) * 1152 + n * 32 + 8 * p4);
            if (g != 0 || nu < 5) f1 = bf16x8{0, 0, 0, 0, 0, 0, 0, 0};
            fd[i][1] = f1; pin(fd[i][0]); pin(fd[i][1]); }
        constexpr unsigned RED2 = 144384, AIMG = 149504, STS = 150528;
        unsigned hsrc = WS_H + xc * 8192 + b1 * 1152 + (32 * wave + 8 * g) * 2, ssrc = WS_H + xc * 8192 + 4096 + ((wave & 1) * 18 + (l31 < 18 ? l31 : 0)) * 8;
        unsigned r2w = RED2 + ((wave * 2 + b1) * 80 + 4 * g) * 4, r2r = RED2 + ((wave & 1) * 80 + (lane < 40 ? lane : 0)) * 4, acw = AIMG + (wave & 1) * 144 + lane * 2, acr = AIMG + b1 * 144 + g * 16;
        unsigned e7w = WS_E7 + (((b1 * 18 + (wave >> 1)) * 128 + a) * 32 + 16 * (wave & 1) + 4 * g) * 4;
        float amask = lane < 8 * nu ? 1.f : 0.f, ok18 = l31 < 18 ? 1.f : 0.f;
        float csg = 0.f, csu = 0.f;
        {
            const bf16x8 ones = asb(v4u{0x3F803F80u, 0x3F803F80u, 0x3F803F80u, 0x3F803F80u});
            f32x4 d[5];
#pragma unroll
            for (int t = 0; t < 5; ++t) { d[t] = mfma16(fg[1][t], ones, mfma16(fg[0][t], ones, z4)); if (wave < 2) d[t] = mfma16(fg[2][t], ones, d[t]); }
            if (l15 < 2) {
#pragma unroll
                for (int t = 0; t < 5; ++t) *(LAS f32x4*)(L + r2w + 64 * t) = d[t]; }
            bar();
#pragma unroll
            for (int w = 0; w < 8; ++w) { csg += *(LAS float*)(L + r2r + w * 640); csu += *(LAS float*)(L + r2r + w * 640 + 160); }
            bar();
        }
        pin(hsrc); pin(ssrc); pin(r2w); pin(r2r); pin(acw); pin(acr); pin(e7w); pin(amask); pin(ok18); pin(csg); pin(csu);
        { v4u hv[3]; v2u sv;
          for (int sp = 0; sp < SPIN_CAP; ++sp) { asm volatile("" ::: "memory");
              hv[0] = ld16(rs, hsrc); hv[1] = ld16(rs, hsrc + 512); bool ok = ok4(hv[0]) && ok4(hv[1]);
              if (wave < 2) { hv[2] = ld16(rs, hsrc + 1024); ok = ok && ok4(hv[2]); }
              if (wave >= 6) { sv = ld8(rs, ssrc); ok = ok && sv[0] != SENT && sv[1] != SENT; }
              if (__all(ok)) break; __builtin_amdgcn_s_sleep(H_SLEEP); }
          f32x4 d[5];
#pragma unroll
          for (int t = 0; t < 5; ++t) d[t] = mfma16(fg[0][t], asb(hv[0]), z4);
#pragma unroll
          for (int t = 0; t < 5; ++t) d[t] = mfma16(fg[1][t], asb(hv[1]), d[t]);
          if (wave < 2) {
#pragma unroll
              for (int t = 0; t < 5; ++t) d[t] = mfma16(fg[2][t], asb(hv[2]), d[t]); }
          if (wave >= 6) { float sm = __uint_as_float(sv[0]) * ok18; float mu = halfsum(sm) * (1.f / 576.f); float dm = sm * (1.f / 32.f) - mu;
              float var = halfsum((__uint_as_float(sv[1]) + 32.f * dm * dm) * ok18) * (1.f / 576.f);
              if (lane == 0) *(LAS v2u*)(L + STS + (wave & 1) * 8) = v2u{__float_as_uint(mu), __float_as_uint(__builtin_amdgcn_rsqf(var + 1e-5f))}; }
          if (l15 < 2) {
#pragma unroll
              for (int t = 0; t < 5; ++t) *(LAS f32x4*)(L + r2w + 64 * t) = d[t]; } }
        bar();
        if (wave < 2) {
            float gs = 0.f, us = 0.f;
            const v2u st = *(LAS v2u*)(L + STS + wave * 8); const float mu = __uint_as_float(st[0]), rsd = __uint_as_float(st[1]);
#pragma unroll
            for (int w = 0; w < 8; ++w) { gs += *(LAS float*)(L + r2r + w * 640); us += *(LAS float*)(L + r2r + w * 640 + 160); }
            gs = (gs - mu * csg) * rsd; us = (us - mu * csu) * rsd;
            float act = gs * __builtin_amdgcn_rcpf(1.f + __expf(-gs)) * us;
            act = amask != 0.f ? act : 0.f;
            *(LAS u16*)(L + acw) = (u16)pk2(act, 0.f); }
        bar();
        { bf16x8 a0 = *(LAS bf16x8*)(L + acr), a1 = *(LAS bf16x8*)(L + acr + 64);
          f32x4 d[5];
#pragma unroll
          for (int i = 0; i < 5; ++i) d[i] = mfma16(fd[i][0], a0, z4);
#pragma unroll
          for (int i = 0; i < 5; ++i) d[i] = mfma16(fd[i][1], a1, d[i]);
          if (l15 < 2) {
#pragma unroll
              for (int i = 0; i < 4; ++i) st16(rs, e7w + i * 65536, nos4(d[i]));
              if (wave < 4) st16(rs, e7w + 4 * 65536, nos4(d[4])); } }
    }
}

extern "C" void kernel_launch(void* const* d_in, const int* in_sizes, int n_in, void* d_out, int out_size, void* d_ws, size_t ws_size, hipStream_t stream) {
    (void)hipFuncSetAttribute((const void*)mk, hipFuncAttributeMaxDynamicSharedMemorySize, LDS_BYTES);
    mk<<<256, NT, LDS_BYTES, stream>>>((const u16*)d_in[0], (const u16*)d_in[1], (const u16*)d_in[2], (const u16*)d_in[3], (const u16*)d_in[4], (const u16*)d_in[5],
                                       (const u16*)d_in[7], (const u16*)d_in[8], (const u16*)d_in[9], (u16*)d_out, (char*)d_ws);
}
```

```cpp
#include <hip/hip_runtime.h>
#include <hip/hip_bf16.h>
#include <stdint.h>

typedef unsigned short u16;
typedef unsigned long long u64;
typedef short bf16x8 __attribute__((ext_vector_type(8)));
typedef short s16x4 __attribute__((ext_vector_type(4)));
typedef float f32x4 __attribute__((ext_vector_type(4)));
typedef float f32x16 __attribute__((ext_vector_type(16)));
typedef unsigned v4u __attribute__((ext_vector_type(4)));
typedef unsigned v2u __attribute__((ext_vector_type(2)));
#define LAS __attribute__((address_space(3)))
#define GAS __attribute__((address_space(1)))
#define DI __device__ __forceinline__

#define NA_ROWS 0
#define NB_ROWS 32

constexpr int SK = 16384, MD = 576, DC = 512, FF = 4608;
constexpr int NT = 512;
constexpr int LDS_BYTES = 163840;
constexpr int AUX = 131072;
constexpr unsigned WS_EPOCH = 0;
constexpr unsigned WS_FLAG = 4096;
constexpr unsigned WS_E1 = 32768;
constexpr unsigned WS_QA = WS_E1 + 262144;
constexpr unsigned WS_CLR = WS_QA + 65536;
constexpr unsigned WS_ST = WS_CLR;
constexpr unsigned WS_E4 = WS_ST + 262144;
constexpr unsigned WS_E5 = WS_E4 + 131072;
constexpr unsigned WS_E7 = WS_E5 + 589824;
constexpr unsigned WS_H = WS_E7 + 589824;
constexpr unsigned WS_CLR_END = WS_H + 65536 + 65536;
constexpr unsigned CLR_PER = (WS_CLR_END - WS_CLR) / 128;
static_assert(CLR_PER * 128 == WS_CLR_END - WS_CLR && CLR_PER % 1024 == 0, "clr");
constexpr unsigned WS_PO = 4194304;
constexpr unsigned WS_DBG = WS_PO + 8388608;
constexpr unsigned SENT = 0xFFFFFFFFu;
constexpr int SPIN_CAP = 400000;

DI float bf2f(u16 v) { return __uint_as_float((unsigned)v << 16); }
DI unsigned pk2(float a, float b) {
    typedef __bf16 bf2 __attribute__((ext_vector_type(2)));
    typedef float f2 __attribute__((ext_vector_type(2)));
    f2 v = {a, b}; bf2 r = __builtin_convertvector(v, bf2); return __builtin_bit_cast(unsigned, r);
}
DI float lo_f(unsigned p);
DI float lo_f(unsigned p) { return __uint_as_float(p << 16); }
DI float hi_f(unsigned p) { return __uint_as_float(p & 0xffff0000u); }
DI unsigned nosent(float v) { return __builtin_elementwise_min(__float_as_uint(v), 0xFFFFFFFEu); }
template <class T> DI void pin(T& v) { asm volatile("" : "+v"(v)); }
DI float rb(float v) { return lo_f(pk2(v, 0.f)); }
DI float ex2(float v) { return __builtin_amdgcn_exp2f(v); }
#define NC_ROWS 32
#define E1_SLEEP 2
#define TOUCH 1
#define G1_EARLYW 1
#define FFN_SLEEP 0
#define FFN_AUX 0
#define SWAP 0
#define NF_EARLY 16
#define NW_EARLY 0
#define NODRAIN 0
#define ONEPHASE 1
#define H_SLEEP 2
#define R_SLEEP 0
#define WARM 0
#define VPRE 4
#define FFN_MAP 0
#define NA1 16
#define NA2 40
#define NB1 48
#define NB2 40
#define G1_SLEEP 0
#define G1_N0 1
#define G1_N1 4
#define G1_F1 1
#define G1_F2 5
DI int sw(int row) { return ((row & 3) << 2) | ((row >> 2) & 3); }
DI void bar() { asm volatile("s_waitcnt lgkmcnt(0)\n\ts_barrier" ::: "memory"); }
DI void glds(const void* g, unsigned lds_, int mode) {
    const unsigned lds = __builtin_amdgcn_readfirstlane(lds_);
    unsigned keep;
    if (mode == 1) asm volatile("s_mov_b32 %0, m0\n\ts_mov_b32 m0, %2\n\ts_nop 0\n\tglobal_load_lds_dwordx4 %1, off nt\n\ts_mov_b32 m0, %0" : "=&s"(keep) : "v"(g), "s"(lds) : "memory");
    else if (mode == 2) asm volatile("s_mov_b32 %0, m0\n\ts_mov_b32 m0, %2\n\ts_nop 0\n\tglobal_load_lds_dwordx4 %1, off sc1\n\ts_mov_b32 m0, %0" : "=&s"(keep) : "v"(g), "s"(lds) : "memory");
    else asm volatile("s_mov_b32 %0, m0\n\ts_mov_b32 m0, %2\n\ts_nop 0\n\tglobal_load_lds_dwordx4 %1, off\n\ts_mov_b32 m0, %0" : "=&s"(keep) : "v"(g), "s"(lds) : "memory");
}
DI void vm0() { asm volatile("s_waitcnt vmcnt(0)" ::: "memory"); }
DI v4u ld16(__amdgpu_buffer_rsrc_t rs, unsigned off) { return __builtin_amdgcn_raw_buffer_load_b128(rs, off, 0, 16); }
DI v2u ld8(__amdgpu_buffer_rsrc_t rs, unsigned off) { return __builtin_amdgcn_raw_buffer_load_b64(rs, off, 0, 16); }
DI void st16(__amdgpu_buffer_rsrc_t rs, unsigned off, v4u v) { __builtin_amdgcn_raw_buffer_store_b128(v, rs, off, 0, 16); }
DI void st8(__amdgpu_buffer_rsrc_t rs, unsigned off, v2u v) { __builtin_amdgcn_raw_buffer_store_b64(v, rs, off, 0, 16); }
DI bool ok4(v4u v) { return v[0] != SENT && v[1] != SENT && v[2] != SENT && v[3] != SENT; }
DI f32x4 mfma16(bf16x8 a, bf16x8 b, f32x4 c) { return __builtin_amdgcn_mfma_f32_16x16x32_bf16(a, b, c, 0, 0, 0); }
template <int P> DI float dpp(float v) { return __int_as_float(__builtin_amdgcn_mov_dpp(__float_as_int(v), P, 0xf, 0xf, true)); }
DI float sum16(float v) { auto r = __builtin_amdgcn_permlane16_swap(__float_as_uint(v), __float_as_uint(v), false, false); return __uint_as_float(r[0]) + __uint_as_float(r[1]); }
DI float sum32(float v) { auto r = __builtin_amdgcn_permlane32_swap(__float_as_uint(v), __float_as_uint(v), false, false); return __uint_as_float(r[0]) + __uint_as_float(r[1]); }
DI float max16(float v) { auto r = __builtin_amdgcn_permlane16_swap(__float_as_uint(v), __float_as_uint(v), false, false); return fmaxf(__uint_as_float(r[0]), __uint_as_float(r[1])); }
DI float max32(float v) { auto r = __builtin_amdgcn_permlane32_swap(__float_as_uint(v), __float_as_uint(v), false, false); return fmaxf(__uint_as_float(r[0]), __uint_as_float(r[1])); }
DI float rowsum(float v) { v += dpp<0x128>(v); v += dpp<0x124>(v); v += dpp<0x122>(v); v += dpp<0x121>(v); return v; }
DI float rowmax(float v) { v = fmaxf(v, dpp<0x128>(v)); v = fmaxf(v, dpp<0x124>(v)); v = fmaxf(v, dpp<0x122>(v)); v = fmaxf(v, dpp<0x121>(v)); return v; }
DI float halfsum(float v) { return sum16(rowsum(v)); }
DI float other_half(float v) { auto r = __builtin_amdgcn_permlane32_swap(__float_as_uint(v), __float_as_uint(v), false, false); return __uint_as_float((threadIdx.x & 32) ? r[0] : r[1]); }
DI f32x16 mfma32(bf16x8 a, bf16x8 b, f32x16 c) { return __builtin_amdgcn_mfma_f32_32x32x16_bf16(a, b, c, 0, 0, 0); }
DI bf16x8 trfrag(LAS char* L, unsigned a0, unsigned a1) {
    s16x4 x = __builtin_amdgcn_ds_read_tr16_b64_v4i16((LAS s16x4*)(L + a0));
    s16x4 y = __builtin_amdgcn_ds_read_tr16_b64_v4i16((LAS s16x4*)(L + a1));
    return __builtin_shufflevector(x, y, 0, 1, 2, 3, 4, 5, 6, 7);
}
DI bf16x8 asb(v4u v) { return __builtin_bit_cast(bf16x8, v); }
DI f32x4 fold(f32x4 d) { return f32x4{d[0] + dpp<0xEE>(d[0]), d[1] + dpp<0xEE>(d[1]), d[2] + dpp<0xEE>(d[2]), d[3] + dpp<0xEE>(d[3])}; }
DI v4u nos4(f32x4 d) { return v4u{nosent(d[0]), nosent(d[1]), nosent(d[2]), nosent(d[3])}; }
DI v4u split8(const float* n, bool lo_part) { v4u o;
#pragma unroll
    for (int j = 0; j < 4; ++j) { unsigned hi = pk2(n[2 * j], n[2 * j + 1]); o[j] = lo_part ? pk2(n[2 * j] - lo_f(hi), n[2 * j + 1] - hi_f(hi)) : hi; }
    return o; }
template <int N> DI void vmw() { asm volatile("s_waitcnt vmcnt(%0)" :: "n"(N) : "memory"); }

#define SB() __builtin_amdgcn_sched_barrier(0)

__global__ __launch_bounds__(NT, 2) void mk(const u16* __restrict__ x, const u16* __restrict__ ckv, const u16* __restrict__ wq, const u16* __restrict__ wuk,
                                           const u16* __restrict__ wuv, const u16* __restrict__ wo, const u16* __restrict__ wg, const u16* __restrict__ wu,
                                           const u16* __restrict__ wd, u16* __restrict__ out, char* __restrict__ ws) {
    extern __shared__ __attribute__((aligned(16))) char smem_[];
    LAS char* L = (LAS char*)smem_;
    const int tid = threadIdx.x, lane = tid & 63, wave = __builtin_amdgcn_readfirstlane(tid >> 6);
    const int g = lane >> 4, l15 = lane & 15, l31 = lane & 31, hf = lane >> 5;
    const int q4 = l15 >> 2, p4 = lane & 3, b1 = lane & 1;
    const int bid = blockIdx.x, xc = bid & 7, jj = bid >> 3;
    const bool isQ = jj < 16;
    const int idx = jj & 15, a = xc * 16 + idx;
    const int hh = xc * 4 + (idx >> 2), rr = idx & 3;
    const int bb = a >> 6, split = (a & 63) + (isQ ? 64 : 0);
    const __amdgpu_buffer_rsrc_t rs = __builtin_amdgcn_make_buffer_rsrc(ws, 0, 0x10000000, 0x00020000);
    const u16* kvg = ckv + ((size_t)bb * SK + split * 128) * DC;
    const f32x4 z4 = {0.f, 0.f, 0.f, 0.f};
    auto dma_kv = [&](int row) { glds(kvg + (size_t)row * DC + ((lane ^ sw(row)) << 3), (unsigned)row * 1024u, 1); };
    unsigned t0a, t0b;
    { int ra = 8 * g + q4, rb = ra + 4, ch = 2 * wave + (p4 >> 1); t0a = ra * 256 + ((ch ^ sw(ra)) << 4) + 8 * (p4 & 1); t0b = rb * 256 + ((ch ^ sw(rb)) << 4) + 8 * (p4 & 1); }
    unsigned tag0, tag1;
    bf16x8 fuv[4];
    constexpr unsigned XRAW = AUX + 20480, EPL = AUX + 24576, QIMG = AUX + 26624, WQI = 88 * 1024, WKI = 96 * 1024;
    if (!isQ) {
        const int nk = rr < 2 ? 5 : 4, row0 = rr < 2 ? 160 * rr : 64 + 128 * rr;
        v4u xf[5], xs[3]; v2u ep;
        if (wave == 7) {
            glds(x + lane * 8, XRAW, 0); glds(x + 512 + lane * 8, XRAW + 1024, 0); glds(x + 1024 + (lane & 15) * 8, XRAW + 2048, 0);
            glds(ws + WS_EPOCH + lane * 16, EPL, 2);
        }
        for (int n = wave; n < 8 * nk; n += 8) { int row = 4 * n + g; glds(wq + (size_t)(row0 + row) * 4096 + hh * 128 + ((l15 ^ sw(row)) << 3), WQI + n * 1024, 1); }
#pragma unroll
        for (int i = 0; i < NA1 / 8; ++i) dma_kv(wave + 8 * i);
        unsigned e1w = WS_E1 + ((hh * 4 + rr) * 2 + b1) * 1024 + (16 * wave + 4 * g + (l15 & 2)) * 8;
        unsigned wa = WQI + t0a, wb = WQI + t0b;
        pin(e1w); pin(wa); pin(wb);
        if (wave == 7) vmw<4 + NA1 / 8>();
        bar();
        ep = *(LAS v2u*)(L + EPL);
        xs[0] = *(LAS v4u*)(L + XRAW + hf * 1152 + l31 * 16); xs[1] = *(LAS v4u*)(L + XRAW + hf * 1152 + 512 + l31 * 16); xs[2] = *(LAS v4u*)(L + XRAW + hf * 1152 + 1024 + (l31 & 7) * 16);
#pragma unroll
        for (int ks = 0; ks < 5; ++ks) xf[ks] = *(LAS v4u*)(L + XRAW + b1 * 1152 + (row0 + 32 * (ks < nk ? ks : 0) + 8 * g) * 2);
        { u64 t = (((u64)ep[1] << 32) | ep[0]) + 1ull; tag0 = (unsigned)t; tag1 = (unsigned)(t >> 32); }
        float mu, rsd;
        {
            const float ok2 = l31 < 8 ? 1.f : 0.f;
            float v[24]; float s = 0.f;
#pragma unroll
            for (int i = 0; i < 3; ++i)
#pragma unroll
                for (int j = 0; j < 4; ++j) { v[8 * i + 2 * j] = lo_f(xs[i][j]); v[8 * i + 2 * j + 1] = hi_f(xs[i][j]); }
            float s2 = 0.f;
#pragma unroll
            for (int i = 0; i < 16; ++i) s += v[i];
#pragma unroll
            for (int i = 16; i < 24; ++i) s2 += v[i];
            mu = halfsum(s + ok2 * s2) * (1.f / 576.f); float q = 0.f, q2 = 0.f;
#pragma unroll
            for (int i = 0; i < 16; ++i) { float d = v[i] - mu; q += d * d; }
#pragma unroll
            for (int i = 16; i < 24; ++i) { float d = v[i] - mu; q2 += d * d; }
            rsd = __builtin_amdgcn_rcpf(rb(__builtin_amdgcn_sqrtf(rb(rb(halfsum(q + ok2 * q2) * (1.f / 576.f)) + 1e-5f)))); mu = rb(mu);
            float mo = other_half(mu), ro = other_half(rsd);
            if (b1 != hf) { mu = mo; rsd = ro; }
            if (l15 == 2) {
#pragma unroll
                for (int ks = 0; ks < 5; ++ks) xf[ks] = v4u{0x3F803F80u, 0x3F803F80u, 0x3F803F80u, 0x3F803F80u}; }
#pragma unroll
            for (int ks = 0; ks < 5; ++ks) pin(xf[ks]);
        }
        unsigned t40lo = tag0 << 24, t40hi = (tag0 >> 8) | (tag1 << 24);
        pin(t40lo); pin(t40hi); pin(mu); pin(rsd);
        vmw<NA1 / 8>();
        bar();
        { f32x4 d = z4;
#pragma unroll
          for (int ks = 0; ks < 4; ++ks) d = mfma16(trfrag(L, wa + ks * 8192, wb + ks * 8192), asb(xf[ks]), d);
          if (nk == 5) d = mfma16(trfrag(L, wa + 32768, wb + 32768), asb(xf[4]), d);
#pragma unroll
          for (int j = 0; j < 4; ++j) d[j] = (d[j] - mu * dpp<0xAA>(d[j])) * rsd;
          float e0 = dpp<0x44>(d[2]), e1 = dpp<0x44>(d[3]);
          if (l15 & 2) { d[0] = e0; d[1] = e1; }
          if (l15 < 4) st16(rs, e1w, v4u{((__float_as_uint(d[0]) + 0x80u) >> 8) | t40lo, t40hi, ((__float_as_uint(d[1]) + 0x80u) >> 8) | t40lo, t40hi}); }
        bar();
        for (int n = wave; n < 32; n += 8) { int e = 4 * n + g; int c = 32 * (e & 3) + 16 * ((e >> 2) & 1) + (e >> 3); glds(wuv + ((size_t)(128 * rr + c) * 32 + hh) * 128 + ((l15 ^ sw(e)) << 3), AUX + n * 1024, 1); }
#pragma unroll
        for (int i = NA1 / 8; i < 16; ++i) { dma_kv(wave + 8 * i); if (i >= G1_F2) vmw<G1_F2>(); }
        bar();
#pragma unroll
        for (int ks = 0; ks < 4; ++ks) { fuv[ks] = trfrag(L, AUX + t0a + ks * 8192 - 65536 + 65536, AUX + t0b + ks * 8192); pin(fuv[ks]); }
        vm0();
    } else {
        if (wave == 7) glds(ws + WS_EPOCH + lane * 16, EPL, 2);
        if (wave == 6) { const char* tp = lane < 8 ? ws + WS_FLAG + lane * 1024 + (a >> 4) * 128 : ws + WS_QA + (((lane & 1) * 32 + hh) * 512 + 128 * rr + ((lane >> 1) & 1) * 64) * 2; glds(tp, EPL + 3072, 2); }
        for (int n = wave; n < 32; n += 8) { int row = 4 * n + g; glds(wuk + ((size_t)(128 * rr + row) * 32 + hh) * 128 + ((l15 ^ sw(row)) << 3), WKI + n * 1024, 1); }
        { v4u s4 = {SENT, SENT, SENT, SENT};
          st16(rs, WS_CLR + a * CLR_PER + wave * 1024 + lane * 16, s4);
          st16(rs, WS_CLR + a * CLR_PER + 8192 + (wave < 5 ? wave : 0) * 1024 + lane * 16, s4); }
#pragma unroll
        for (int ks = 0; ks < 4; ++ks) fuv[ks] = bf16x8{0, 0, 0, 0, 0, 0, 0, 0};
#pragma unroll
        for (int i = 0; i < NA2 / 8; ++i) dma_kv(wave + 8 * i);
        unsigned e1r = WS_E1 + ((hh * 4 + g) * 2 + (l15 >> 3)) * 1024 + (16 * wave + 2 * (lane & 7)) * 8;
        unsigned qw = QIMG + (l15 >> 3) * 272 + (16 * wave + 2 * (lane & 7)) * 2;
        unsigned ka0, ka1, qr, qaw;
        { const int c0 = 32 * (wave & 3) + 8 * q4 + (l15 & 3), c1 = c0 + 4; ka0 = WKI + c0 * 256 + ((g ^ sw(c0)) << 4); ka1 = WKI + c1 * 256 + ((g ^ sw(c1)) << 4);
          qr = QIMG + b1 * 272 + 16 * g; qaw = WS_QA + ((b1 * 32 + hh) * 512 + 128 * rr + 32 * (wave & 3) + 8 * g) * 2; }
        pin(e1r); pin(qw); pin(ka0); pin(ka1); pin(qr); pin(qaw);
        vm0();
        bar();
        { v2u ep = *(LAS v2u*)(L + EPL); u64 t = (((u64)ep[1] << 32) | ep[0]) + 1ull; tag0 = (unsigned)t; tag1 = (unsigned)(t >> 32); }
        const unsigned t40lo = tag0 << 24, t40hi = (tag0 >> 8) | (tag1 << 24);
        { v4u v; bool okv = false;
          for (int sp = 0; sp < SPIN_CAP; ++sp) { asm volatile("" ::: "memory");
              if (!okv) v = ld16(rs, e1r);
              okv = (v[1] == t40hi) & (v[3] == t40hi) & ((v[0] & 0xFF000000u) == t40lo) & ((v[2] & 0xFF000000u) == t40lo);
              if (__all(okv)) break; if (E1_SLEEP) __builtin_amdgcn_s_sleep(E1_SLEEP); }
          float q0 = sum32(sum16(__uint_as_float(v[0] << 8))), q1 = sum32(sum16(__uint_as_float(v[2] << 8)));
          if (lane < 16) *(LAS unsigned*)(L + qw) = pk2(q0, q1);
        }
        bar();
        if (wave < 4) {
            f32x4 d0 = z4, d1 = z4;
#pragma unroll
            for (int ks = 0; ks < 4; ++ks) { bf16x8 bq = *(LAS bf16x8*)(L + qr + 64 * ks);
                d0 = mfma16(*(LAS bf16x8*)(L + (ka0 ^ (ks << 6))), bq, d0); d1 = mfma16(*(LAS bf16x8*)(L + (ka1 ^ (ks << 6))), bq, d1); }
            if (l15 < 2) st16(rs, qaw, v4u{pk2(d0[0], d0[1]), pk2(d0[2], d0[3]), pk2(d1[0], d1[1]), pk2(d1[2], d1[3])});
            vm0();
        }
        bar();
        if (wave == 0 && lane < 8) st8(rs, WS_FLAG + lane * 1024 + a * 8, v2u{tag0, tag1});
        if (wave >= 4) for (int r = NA2 + wave - 4; r < NA2 + NB2; r += 4) dma_kv(r);
    }
    const int NAr = isQ ? NA2 : 128, NBr = isQ ? NB2 : 0;
    unsigned a8[8];
#pragma unroll
    for (int j = 0; j < 8; ++j) { a8[j] = AUX + l31 * 1024 + (((2 * j + hf) ^ sw(l31)) << 4); pin(a8[j]); }
    if (wave == 0) {
        for (int sp = 0; sp < SPIN_CAP; ++sp) { asm volatile("" ::: "memory"); v4u f = ld16(rs, WS_FLAG + xc * 1024 + lane * 16);
            if (__all(f[0] == tag0 && f[1] == tag1 && f[2] == tag0 && f[3] == tag1)) break; }
    }
    bar();
    if (wave < 4) for (int r = wave; r < 32; r += 4) glds(ws + WS_QA + (bb * 32 + r) * 1024 + ((lane ^ sw(r)) << 4), AUX + r * 1024, 2);
    bar();
    if (wave < 4) vm0(); else for (int r = NAr + NBr + wave - 4; r < NAr + NBr + NC_ROWS && r < 128; r += 4) dma_kv(r);
    bar();
    if (wave >= 4) for (int r = NAr + NBr + NC_ROWS + wave - 4; r < 128; r += 4) dma_kv(r);
    constexpr unsigned WOI = 32768, PIM = 69632, SCR = 135168, WDI_ = 92160;
    const int af_ = FFN_MAP ? idx * 8 + xc : a;
    const int u0_ = (9 * af_) >> 1, nu_ = ((9 * (af_ + 1)) >> 1) - u0_;
    auto dma_gu = [&](int n) { int q = n * 64 + lane; int row = q / 10, k = q - row * 10; int ku = k >= 5 ? k - 5 : k; if (ku >= nu_) ku = nu_ - 1;
        glds((k >= 5 ? wu : wg) + (size_t)row * FF + (u0_ + ku) * 8, n * 1024, FFN_AUX); };
    auto dma_cw = [&](int n) { glds(wo + (size_t)(hh * 128 + 32 * rr) * MD + n * 512 + lane * 8, WOI + n * 1024, 1); };
    {
        LAS float* mx = (LAS float*)(L + AUX);
        LAS float* lsum = (LAS float*)(L + AUX + 512);
        LAS char* pim = L + AUX + 1024;
        if (wave < 4) {
            bf16x8 qf[32], kf[16];
#pragma unroll
            for (int s = 0; s < 32; ++s) qf[s] = *(LAS bf16x8*)(L + a8[s & 7] + (s >> 3) * 256);
#pragma unroll
            for (int j = 0; j < 8; ++j) { a8[j] += (unsigned)(wave * 32768) - AUX; pin(a8[j]); }
            unsigned pw = AUX + 1024 + l31 * 272 + (32 * wave + 4 * hf) * 2; pin(pw);
            vm0();
            bar();
            f32x16 s;
#pragma unroll
            for (int i = 0; i < 16; ++i) s[i] = 0.f;
#pragma unroll
            for (int st = 0; st < 16; ++st) kf[st] = *(LAS bf16x8*)(L + a8[st & 7] + (st >> 3) * 256);
            SB();
#pragma unroll
            for (int st = 0; st < 16; ++st) { s = mfma32(kf[st], qf[st], s); SB(); kf[st] = *(LAS bf16x8*)(L + a8[st & 7] + (2 + (st >> 3)) * 256); SB(); }
#pragma unroll
            for (int st = 0; st < 16; ++st) s = mfma32(kf[st], qf[16 + st], s);
#pragma unroll
            for (int i = 0; i < 16; i += 2) { unsigned r1 = pk2(s[i], s[i + 1]); unsigned r2 = pk2(lo_f(r1) * 0.08838834764831845f, hi_f(r1) * 0.08838834764831845f);
                s[i] = lo_f(r2) * 1.4426950408889634f; s[i + 1] = hi_f(r2) * 1.4426950408889634f; }
            float m = s[0];
#pragma unroll
            for (int i = 1; i < 16; ++i) m = fmaxf(m, s[i]);
            m = max32(m);
            if (lane < 32) mx[wave * 32 + lane] = m;
            bar();
            const float M = fmaxf(fmaxf(mx[l31], mx[32 + l31]), fmaxf(mx[64 + l31], mx[96 + l31]));
            float ls = 0.f;
#pragma unroll
            for (int gq = 0; gq < 4; ++gq) { float e0 = ex2(s[4 * gq] - M), e1 = ex2(s[4 * gq + 1] - M), e2 = ex2(s[4 * gq + 2] - M), e3 = ex2(s[4 * gq + 3] - M);
                *(LAS v2u*)(L + pw + 16 * gq) = v2u{pk2(e0, e1), pk2(e2, e3)}; ls += (e0 + e1) + (e2 + e3); }
            ls = sum32(ls);
            if (lane < 32) lsum[wave * 32 + lane] = ls;
            bar();
            if (isQ) { for (int n = wave; n < NF_EARLY; n += 4) dma_gu(n); } else { for (int n = wave; n < NW_EARLY; n += 4) dma_cw(n); }
            bar();
        } else {
            const int cq = wave - 4, blk = g & 1;
            unsigned va[4], vb[4];
#pragma unroll
            for (int t = 0; t < 4; ++t) { const int ra = 8 * hf + q4, rb = ra + 4; int ch = 16 * cq + 4 * t + 2 * blk + (p4 >> 1);
                va[t] = ra * 1024 + ((ch ^ sw(ra)) << 4) + 8 * (p4 & 1); vb[t] = rb * 1024 + ((ch ^ sw(rb)) << 4) + 8 * (p4 & 1); pin(va[t]); pin(vb[t]); }
            unsigned pr = AUX + 1024 + l31 * 272 + 16 * hf, po = WS_PO + ((((bb * 32 + 4 * hf + blk) * 4 + cq) * 128 + split) * 16 + l15) * 16; pin(pr); pin(po);
            vm0();
            bar();
            bf16x8 vf[8][4];
#pragma unroll
            for (int ks = 0; ks < 4; ++ks)
#pragma unroll
                for (int t = 0; t < 4; ++t) vf[ks][t] = trfrag(L, va[t] + ks * 16384, vb[t] + ks * 16384);
#pragma unroll
            for (int t = 0; t < 4; ++t) { va[t] += 65536; vb[t] += 65536; }
#pragma unroll
            for (int ks = 0; ks < VPRE; ++ks)
#pragma unroll
                for (int t = 0; t < 4; ++t) vf[4 + ks][t] = trfrag(L, va[t] + ks * 16384, vb[t] + ks * 16384);
            bar();
#pragma unroll
            for (int ks = VPRE; ks < 4; ++ks)
#pragma unroll
                for (int t = 0; t < 4; ++t) vf[4 + ks][t] = trfrag(L, va[t] + ks * 16384, vb[t] + ks * 16384);
            bar();
            f32x16 o[4];
#pragma unroll
            for (int t = 0; t < 4; ++t)
#pragma unroll
                for (int i = 0; i < 16; ++i) o[t][i] = 0.f;
            bf16x8 pa[8];
#pragma unroll
            for (int ks = 0; ks < 8; ++ks) pa[ks] = *(LAS bf16x8*)(L + pr + 32 * ks);
#pragma unroll
            for (int ks = 0; ks < 8; ++ks)
#pragma unroll
                for (int t = 0; t < 4; ++t) o[t] = mfma32(pa[ks], vf[ks][t], o[t]);
#pragma unroll
            for (int r = 0; r < 16; r += 2) {
                auto x0 = __builtin_amdgcn_permlane16_swap(pk2(o[0][r], o[1][r]), pk2(o[0][r + 1], o[1][r + 1]), false, false);
                auto x1 = __builtin_amdgcn_permlane16_swap(pk2(o[2][r], o[3][r]), pk2(o[2][r + 1], o[3][r + 1]), false, false);
                st16(rs, po + ((r & 3) + 8 * (r >> 2)) * 131072, v4u{x0[0], x1[0], x0[1], x1[1]}); }
            v2u sv = {nosent(fmaxf(fmaxf(mx[l31], mx[32 + l31]), fmaxf(mx[64 + l31], mx[96 + l31]))), nosent((lsum[l31] + lsum[32 + l31]) + (lsum[64 + l31] + lsum[96 + l31]))};
            unsigned so = WS_ST + (((bb * 32 + l31) * 4 + cq) * 128 + split) * 8; pin(sv); pin(so);
            if (!(NODRAIN && isQ)) vm0();
            if (lane < 32) st8(rs, so, sv);
            bar();
        }
    }
    if (isQ == (SWAP != 0)) {
        for (int n = NW_EARLY + wave; n < 36; n += 8) dma_cw(n);
        vm0();
        bar();
        bf16x8 fo[5];
#pragma unroll
        for (int i = 0; i < 5; ++i) { int n = wave + 8 * i; if (n > 35) n = 35; int ra = 8 * g + q4;
            fo[i] = trfrag(L, WOI + ra * 1152 + n * 32 + 8 * p4, WOI + (ra + 4) * 1152 + n * 32 + 8 * p4); pin(fo[i]); }
        constexpr unsigned WIMG = SCR, LINV = SCR + 1280, AIMG = SCR + 1536, AIMG2 = SCR + 2816, R3 = SCR + 3328;
        unsigned pa_ = PIM + t0a, pb_ = PIM + t0b, wr = WIMG + b1 * 272 + 16 * g, aw = AIMG + b1 * 272 + (16 * wave + 4 * g) * 2, ar = AIMG + b1 * 272 + 16 * g;
        unsigned e4w = WS_E4 + ((((hh * 4 + (wave >> 1)) * 4 + rr) * 2 + b1) * 32 + 16 * (wave & 1) + 4 * g) * 4, e4r = WS_E4 + (hh * 4 + rr) * 1024 + lane * 16;
        unsigned a2w = AIMG2 + (l15 >> 3) * 80 + (lane & 7) * 8, a2r = AIMG2 + b1 * 80 + g * 16;
        unsigned e5w = WS_E5 + (((b1 * 18 + (wave >> 1)) * 128 + a) * 32 + 16 * (wave & 1) + 4 * g) * 4;
        pin(pa_); pin(pb_); pin(wr); pin(aw); pin(ar); pin(e4w); pin(e4r); pin(a2w); pin(a2r); pin(e5w);
        v2u sv[2];
        const char* src[4];
        if (tid == 0) *(LAS unsigned*)(L + LINV + 16) = 0u;
        {
#pragma unroll
            for (int k = 0; k < 4; ++k) { int n = wave + 8 * k; int b = n >> 4, row = 64 + 4 * (n & 15) + g; src[k] = ws + WS_PO + ((b * 32 + hh) * 4 + rr) * 32768 + row * 256 + ((l15 ^ sw(row)) << 4); pin(src[k]); }
            if (wave < 2) { const unsigned so_ = WS_ST + (((wave * 32 + hh) * 4 + rr) * 128 + lane) * 8;
                for (int sp = 0; sp < SPIN_CAP; ++sp) { asm volatile("" ::: "memory"); sv[0] = ld8(rs, so_); sv[1] = ld8(rs, so_ + 512); if (__all(sv[0][0] != SENT && sv[0][1] != SENT && sv[1][0] != SENT && sv[1][1] != SENT)) break; }
            }
            bar();
#pragma unroll
            for (int k = 0; k < 4; ++k) { int n = wave + 8 * k; glds(src[k] - 16384, PIM + (n >> 4) * 32768 + (n & 15) * 1024, 2); }
#pragma unroll
            for (int k = 0; k < 4; ++k) { int n = wave + 8 * k; glds(src[k], PIM + (n >> 4) * 32768 + (16 + (n & 15)) * 1024, 2); }
        }
        if (wave < 2) { float m0 = __uint_as_float(sv[0][0]), m1 = __uint_as_float(sv[1][0]);
            float M = max32(max16(rowmax(fmaxf(m0, m1))));
            unsigned wp = pk2(ex2(m0 - M), ex2(m1 - M));
            float Ls = sum32(halfsum(lo_f(wp) * __uint_as_float(sv[0][1]) + hi_f(wp) * __uint_as_float(sv[1][1])));
            *(LAS u16*)(L + WIMG + wave * 272 + lane * 2) = (u16)wp; *(LAS u16*)(L + WIMG + wave * 272 + 128 + lane * 2) = (u16)(wp >> 16);
            if (lane == 0) *(LAS float*)(L + LINV + wave * 4) = __builtin_amdgcn_rcpf(Ls); }
        for (unsigned it = 1;; ++it) {
            vm0();
            bar();
            f32x4 d0 = z4, d1 = z4;
            typedef unsigned short us2 __attribute__((ext_vector_type(2)));
            us2 mxu = {0, 0};
#pragma unroll
            for (int ks = 0; ks < 4; ++ks) { bf16x8 bw = *(LAS bf16x8*)(L + wr + 64 * ks);
                bf16x8 f0 = trfrag(L, pa_ + ks * 8192, pb_ + ks * 8192), f1 = trfrag(L, pa_ + 32768 + ks * 8192, pb_ + 32768 + ks * 8192);
                d0 = mfma16(f0, bw, d0); d1 = mfma16(f1, bw, d1);
                if (NODRAIN && ks >= 2) { v4u u0 = __builtin_bit_cast(v4u, f0), u1 = __builtin_bit_cast(v4u, f1);
#pragma unroll
                    for (int j = 0; j < 4; ++j) { mxu = __builtin_elementwise_max(mxu, __builtin_bit_cast(us2, u0[j])); mxu = __builtin_elementwise_max(mxu, __builtin_bit_cast(us2, u1[j])); } } }
            const float inv = *(LAS float*)(L + LINV + b1 * 4);
            f32x4 r = (b1 ? d1 : d0) * inv;
            if (l15 < 2) *(LAS v2u*)(L + aw) = v2u{pk2(r[0], r[1]), pk2(r[2], r[3])};
            if (NODRAIN && (mxu[0] == 0xFFFFu || mxu[1] == 0xFFFFu)) *(LAS unsigned*)(L + LINV + 16) = it;
            bar();
            if (!NODRAIN) break;
            if (__builtin_amdgcn_readfirstlane(*(LAS unsigned*)(L + LINV + 16)) != it || it > 2000u) break;
#pragma unroll
            for (int k = 0; k < 4; ++k) { int n = wave + 8 * k; glds(src[k], PIM + (n >> 4) * 32768 + (16 + (n & 15)) * 1024, 2); }
        }
        { f32x4 d = z4;
#pragma unroll
          for (int ks = 0; ks < 4; ++ks) d = mfma16(fuv[ks], *(LAS bf16x8*)(L + ar + 64 * ks), d);
          if (l15 < 2) st16(rs, e4w, nos4(d)); }
        if (wave == 0) { v4u v;
            for (int sp = 0; sp < SPIN_CAP; ++sp) { asm volatile("" ::: "memory"); v = ld16(rs, e4r); if (__all(ok4(v))) break; }
            float c[4];
#pragma unroll
            for (int j = 0; j < 4; ++j) c[j] = sum32(sum16(__uint_as_float(v[j])));
            if (lane < 16) *(LAS v2u*)(L + a2w) = v2u{pk2(c[0], c[1]), pk2(c[2], c[3])}; }
        bar();
        { bf16x8 av = *(LAS bf16x8*)(L + a2r);
          f32x4 d[5];
#pragma unroll
          for (int i = 0; i < 5; ++i) d[i] = mfma16(fo[i], av, z4);
          if (l15 < 2) {
#pragma unroll
              for (int i = 0; i < 4; ++i) st16(rs, e5w + i * 65536, nos4(d[i]));
              if (wave < 4) st16(rs, e5w + 4 * 65536, nos4(d[4])); } }
        const int rho = xc + 8 * idx;
        if (rho < 36) {
            LAS float* r3 = (LAS float*)(L + R3);
            f32x4 hv;
            const v2u xv = *(const v2u*)(x + rho * 32 + (lane & 7) * 4);
#pragma unroll
            for (int ph = 0; ph < 2; ++ph) {
                const unsigned src = (ph ? WS_E7 : WS_E5) + rho * 16384 + tid * 16;
                v4u v0, v1;
                bool k0 = false, k1 = false;
                for (int sp = 0; sp < SPIN_CAP; ++sp) { asm volatile("" ::: "memory"); if (!k0) v0 = ld16(rs, src); if (!k1) v1 = ld16(rs, src + 8192); k0 = ok4(v0); k1 = ok4(v1); if (__all(k0 && k1)) break; if (R_SLEEP) __builtin_amdgcn_s_sleep(R_SLEEP); }
                f32x4 s;
#pragma unroll
                for (int j = 0; j < 4; ++j) { float t = __uint_as_float(v0[j]) + __uint_as_float(v1[j]); t += dpp<0x128>(t); s[j] = sum32(sum16(t)); }
                if (lane < 8) *(LAS f32x4*)(r3 + wave * 32 + lane * 4) = s;
                bar();
                if (wave == 0) { const int q = lane & 7; f32x4 t = z4;
#pragma unroll
                    for (int w = 0; w < 8; ++w) t += *(LAS f32x4*)(r3 + w * 32 + q * 4);
                    if (ph == 0) {
                        { unsigned a0 = pk2(t[0], t[1]), a1 = pk2(t[2], t[3]); hv = f32x4{lo_f(a0) + lo_f(xv[0]), hi_f(a0) + hi_f(xv[0]), lo_f(a1) + lo_f(xv[1]), hi_f(a1) + hi_f(xv[1])}; }
                        unsigned h0 = pk2(hv[0], hv[1]), h1 = pk2(hv[2], hv[3]);
                        hv = f32x4{lo_f(h0), hi_f(h0), lo_f(h1), hi_f(h1)};
                        st8(rs, WS_H + (lane >> 3) * 8192 + rho * 64 + q * 8, v2u{__builtin_elementwise_min(h0, 0xFFFFFFFEu), __builtin_elementwise_min(h1, 0xFFFFFFFEu)});
                        f32x4 r = {lo_f(h0), hi_f(h0), lo_f(h1), hi_f(h1)};
                        float sm = (r[0] + r[1]) + (r[2] + r[3]); sm += dpp<0xB1>(sm); sm += dpp<0x4E>(sm); sm += dpp<0x141>(sm);
                        r -= sm * (1.f / 32.f);
                        float m2 = (r[0] * r[0] + r[1] * r[1]) + (r[2] * r[2] + r[3] * r[3]); m2 += dpp<0xB1>(m2); m2 += dpp<0x4E>(m2); m2 += dpp<0x141>(m2);
                        if (q == 0) st8(rs, WS_H + (lane >> 3) * 8192 + 4096 + rho * 8, v2u{nosent(sm), nosent(m2)});
                        if (rho == 0 && lane == 0) st8(rs, WS_EPOCH, v2u{tag0, tag1});
                    } else if (lane < 8) { unsigned a0 = pk2(t[0], t[1]), a1 = pk2(t[2], t[3]); *(v2u*)(out + rho * 32 + q * 4) = v2u{pk2(hv[0] + lo_f(a0), hv[1] + hi_f(a0)), pk2(hv[2] + lo_f(a1), hv[3] + hi_f(a1))}; } }
                bar();
            }
        }
    } else {
        const int u0 = u0_, nu = nu_;
        constexpr unsigned WDI = 92160;
        if (FFN_SLEEP) __builtin_amdgcn_s_sleep(FFN_SLEEP);
        {
            for (int n = NF_EARLY + wave; n < 90; n += 8) dma_gu(n);
            for (int n = wave; n < 9 * nu; n += 8) glds(wd + (size_t)u0 * 8 * MD + n * 512 + lane * 8, WDI + n * 1024, 1);
        }
        vm0();
        bar();
        bf16x8 fg[3][5], fd[5][2];
#pragma unroll
        for (int kk = 0; kk < 3; ++kk) { int ks = wave + 8 * kk; if (ks > 17) ks = 17; int ra = 32 * ks + 8 * g + q4;
#pragma unroll
            for (int t = 0; t < 5; ++t) { fg[kk][t] = trfrag(L, ra * 160 + t * 32 + 8 * p4, (ra + 4) * 160 + t * 32 + 8 * p4); pin(fg[kk][t]); } }
#pragma unroll
        for (int i = 0; i < 5; ++i) { int n = wave + 8 * i; if (n > 35) n = 35; int ra = 8 * g + q4;
            fd[i][0] = trfrag(L, WDI + ra * 1152 + n * 32 + 8 * p4, WDI + (ra + 4) * 1152 + n * 32 + 8 * p4);
            int rc = 32 + q4;
            bf16x8 f1 = trfrag(L, WDI + rc * 1152 + n * 32 + 8 * p4, WDI + (rc + 4) * 1152 + n * 32 + 8 * p4);
            if (g != 0 || nu < 5) f1 = bf16x8{0, 0, 0, 0, 0, 0, 0, 0};
            fd[i][1] = f1; pin(fd[i][0]); pin(fd[i][1]); }
        constexpr unsigned RED2 = 144384, AIMG = 149504, STS = 150528;
        unsigned hsrc = WS_H + xc * 8192 + b1 * 1152 + (32 * wave + 8 * g) * 2, ssrc = WS_H + xc * 8192 + 4096 + ((wave & 1) * 18 + (l31 < 18 ? l31 : 0)) * 8;
        unsigned r2w = RED2 + ((wave * 2 + b1) * 80 + 4 * g) * 4, r2r = RED2 + ((wave & 1) * 80 + (lane < 40 ? lane : 0)) * 4, acw = AIMG + (wave & 1) * 144 + lane * 2, acr = AIMG + b1 * 144 + g * 16;
        unsigned e7w = WS_E7 + (((b1 * 18 + (wave >> 1)) * 128 + a) * 32 + 16 * (wave & 1) + 4 * g) * 4;
        float amask = lane < 8 * nu ? 1.f : 0.f, ok18 = l31 < 18 ? 1.f : 0.f;
        float csg = 0.f, csu = 0.f;
        {
            const bf16x8 ones = asb(v4u{0x3F803F80u, 0x3F803F80u, 0x3F803F80u, 0x3F803F80u});
            f32x4 d[5];
#pragma unroll
            for (int t = 0; t < 5; ++t) { d[t] = mfma16(fg[1][t], ones, mfma16(fg[0][t], ones, z4)); if (wave < 2) d[t] = mfma16(fg[2][t], ones, d[t]); }
            if (l15 < 2) {
#pragma unroll
                for (int t = 0; t < 5; ++t) *(LAS f32x4*)(L + r2w + 64 * t) = d[t]; }
            bar();
#pragma unroll
            for (int w = 0; w < 8; ++w) { csg += *(LAS float*)(L + r2r + w * 640); csu += *(LAS float*)(L + r2r + w * 640 + 160); }
            bar();
        }
        pin(hsrc); pin(ssrc); pin(r2w); pin(r2r); pin(acw); pin(acr); pin(e7w); pin(amask); pin(ok18); pin(csg); pin(csu);
        { v4u hv[3]; v2u sv;
          for (int sp = 0; sp < SPIN_CAP; ++sp) { asm volatile("" ::: "memory");
              hv[0] = ld16(rs, hsrc); hv[1] = ld16(rs, hsrc + 512); bool ok = ok4(hv[0]) && ok4(hv[1]);
              if (wave < 2) { hv[2] = ld16(rs, hsrc + 1024); ok = ok && ok4(hv[2]); }
              if (wave >= 6) { sv = ld8(rs, ssrc); ok = ok && sv[0] != SENT && sv[1] != SENT; }
              if (__all(ok)) break; __builtin_amdgcn_s_sleep(H_SLEEP); }
          f32x4 d[5];
#pragma unroll
          for (int t = 0; t < 5; ++t) d[t] = mfma16(fg[0][t], asb(hv[0]), z4);
#pragma unroll
          for (int t = 0; t < 5; ++t) d[t] = mfma16(fg[1][t], asb(hv[1]), d[t]);
          if (wave < 2) {
#pragma unroll
              for (int t = 0; t < 5; ++t) d[t] = mfma16(fg[2][t], asb(hv[2]), d[t]); }
          if (wave >= 6) { float sm = __uint_as_float(sv[0]) * ok18; float mu = halfsum(sm) * (1.f / 576.f); float dm = sm * (1.f / 32.f) - mu;
              float var = halfsum((__uint_as_float(sv[1]) + 32.f * dm * dm) * ok18) * (1.f / 576.f);
              if (lane == 0) *(LAS v2u*)(L + STS + (wave & 1) * 8) = v2u{__float_as_uint(rb(mu)), __float_as_uint(__builtin_amdgcn_rcpf(rb(__builtin_amdgcn_sqrtf(rb(rb(var) + 1e-5f)))))}; }
          if (l15 < 2) {
#pragma unroll
              for (int t = 0; t < 5; ++t) *(LAS f32x4*)(L + r2w + 64 * t) = d[t]; } }
        bar();
        if (wave < 2) {
            float gs = 0.f, us = 0.f;
            const v2u st = *(LAS v2u*)(L + STS + wave * 8); const float mu = __uint_as_float(st[0]), rsd = __uint_as_float(st[1]);
#pragma unroll
            for (int w = 0; w < 8; ++w) { gs += *(LAS float*)(L + r2r + w * 640); us += *(LAS float*)(L + r2r + w * 640 + 160); }
            gs = rb((gs - mu * csg) * rsd); us = rb((us - mu * csu) * rsd);
            float act = rb(gs * __builtin_amdgcn_rcpf(1.f + __expf(-gs))) * us;
            act = amask != 0.f ? act : 0.f;
            *(LAS u16*)(L + acw) = (u16)pk2(act, 0.f); }
        bar();
        { bf16x8 a0 = *(LAS bf16x8*)(L + acr), a1 = *(LAS bf16x8*)(L + acr + 64);
          f32x4 d[5];
#pragma unroll
          for (int i = 0; i < 5; ++i) d[i] = mfma16(fd[i][0], a0, z4);
#pragma unroll
          for (int i = 0; i < 5; ++i) d[i] = mfma16(fd[i][1], a1, d[i]);
          if (l15 < 2) {
#pragma unroll
              for (int i = 0; i < 4; ++i) st16(rs, e7w + i * 65536, nos4(d[i]));
              if (wave < 4) st16(rs, e7w + 4 * 65536, nos4(d[4])); } }
    }
}

extern "C" void kernel_launch(void* const* d_in, const int* in_sizes, int n_in, void* d_out, int out_size, void* d_ws, size_t ws_size, hipStream_t stream) {
    (void)hipFuncSetAttribute((const void*)mk, hipFuncAttributeMaxDynamicSharedMemorySize, LDS_BYTES);
    mk<<<256, NT, LDS_BYTES, stream>>>((const u16*)d_in[0], (const u16*)d_in[1], (const u16*)d_in[2], (const u16*)d_in[3], (const u16*)d_in[4], (const u16*)d_in[5],
                                       (const u16*)d_in[7], (const u16*)d_in[8], (const u16*)d_in[9], (u16*)d_out, (char*)d_ws);
}
```

```cpp
#include <hip/hip_runtime.h>
#include <hip/hip_bf16.h>
#include <stdint.h>

typedef unsigned short u16;
typedef unsigned long long u64;
typedef short bf16x8 __attribute__((ext_vector_type(8)));
typedef short s16x4 __attribute__((ext_vector_type(4)));
typedef float f32x4 __attribute__((ext_vector_type(4)));
typedef float f32x16 __attribute__((ext_vector_type(16)));
typedef unsigned v4u __attribute__((ext_vector_type(4)));
typedef unsigned v2u __attribute__((ext_vector_type(2)));
#define LAS __attribute__((address_space(3)))
#define GAS __attribute__((address_space(1)))
#define DI __device__ __forceinline__

#define NA_ROWS 0
#define NB_ROWS 32

constexpr int SK = 16384, MD = 576, DC = 512, FF = 4608;
constexpr int NT = 512;
constexpr int LDS_BYTES = 163840;
constexpr int AUX = 131072;
constexpr unsigned WS_EPOCH = 0;
constexpr unsigned WS_FLAG = 4096;
constexpr unsigned WS_E1 = 32768;
constexpr unsigned WS_QA = WS_E1 + 262144;
constexpr unsigned WS_CLR = WS_QA + 65536;
constexpr unsigned WS_ST = WS_CLR;
constexpr unsigned WS_E4 = WS_ST + 262144;
constexpr unsigned WS_E5 = WS_E4 + 131072;
constexpr unsigned WS_E7 = WS_E5 + 589824;
constexpr unsigned WS_H = WS_E7 + 589824;
constexpr unsigned WS_CLR_END = WS_H + 65536 + 65536;
constexpr unsigned CLR_PER = (WS_CLR_END - WS_CLR) / 128;
static_assert(CLR_PER * 128 == WS_CLR_END - WS_CLR && CLR_PER % 1024 == 0, "clr");
constexpr unsigned WS_PO = 4194304;
constexpr unsigned WS_DBG = WS_PO + 8388608;
constexpr unsigned SENT = 0xFFFFFFFFu;
constexpr int SPIN_CAP = 400000;

DI float bf2f(u16 v) { return __uint_as_float((unsigned)v << 16); }
DI unsigned pk2(float a, float b) {
    typedef __bf16 bf2 __attribute__((ext_vector_type(2)));
    typedef float f2 __attribute__((ext_vector_type(2)));
    f2 v = {a, b}; bf2 r = __builtin_convertvector(v, bf2); return __builtin_bit_cast(unsigned, r);
}
DI float lo_f(unsigned p);
DI float lo_f(unsigned p) { return __uint_as_float(p << 16); }
DI float hi_f(unsigned p) { return __uint_as_float(p & 0xffff0000u); }
DI unsigned nosent(float v) { return __builtin_elementwise_min(__float_as_uint(v), 0xFFFFFFFEu); }
template <class T> DI void pin(T& v) { asm volatile("" : "+v"(v)); }
DI float rb(float v) { return lo_f(pk2(v, 0.f)); }
DI float ex2(float v) { return __builtin_amdgcn_exp2f(v); }
#define NC_ROWS 32
#define E1_SLEEP 2
#define TOUCH 1
#define G1_EARLYW 1
#define FFN_SLEEP 0
#define FFN_AUX 0
#define SWAP 0
#define NF_EARLY 16
#define NW_EARLY 0
#define NODRAIN 0
#define ONEPHASE 1
#define H_SLEEP 2
#define R_SLEEP 0
#define WARM 0
#define VPRE 4
#define FFN_MAP 0
#define NA1 16
#define NA2 40
#define NB1 48
#define NB2 40
#define G1_SLEEP 0
#define G1_N0 1
#define G1_N1 4
#define G1_F1 1
#define G1_F2 5
DI int sw(int row) { return ((row & 3) << 2) | ((row >> 2) & 3); }
DI void bar() { asm volatile("s_waitcnt lgkmcnt(0)\n\ts_barrier" ::: "memory"); }
DI void glds(const void* g, unsigned lds_, int mode) {
    const unsigned lds = __builtin_amdgcn_readfirstlane(lds_);
    unsigned keep;
    if (mode == 1) asm volatile("s_mov_b32 %0, m0\n\ts_mov_b32 m0, %2\n\ts_nop 0\n\tglobal_load_lds_dwordx4 %1, off nt\n\ts_mov_b32 m0, %0" : "=&s"(keep) : "v"(g), "s"(lds) : "memory");
    else if (mode == 2) asm volatile("s_mov_b32 %0, m0\n\ts_mov_b32 m0, %2\n\ts_nop 0\n\tglobal_load_lds_dwordx4 %1, off sc1\n\ts_mov_b32 m0, %0" : "=&s"(keep) : "v"(g), "s"(lds) : "memory");
    else asm volatile("s_mov_b32 %0, m0\n\ts_mov_b32 m0, %2\n\ts_nop 0\n\tglobal_load_lds_dwordx4 %1, off\n\ts_mov_b32 m0, %0" : "=&s"(keep) : "v"(g), "s"(lds) : "memory");
}
DI void vm0() { asm volatile("s_waitcnt vmcnt(0)" ::: "memory"); }
DI v4u ld16(__amdgpu_buffer_rsrc_t rs, unsigned off) { return __builtin_amdgcn_raw_buffer_load_b128(rs, off, 0, 16); }
DI v2u ld8(__amdgpu_buffer_rsrc_t rs, unsigned off) { return __builtin_amdgcn_raw_buffer_load_b64(rs, off, 0, 16); }
DI void st16(__amdgpu_buffer_rsrc_t rs, unsigned off, v4u v) { __builtin_amdgcn_raw_buffer_store_b128(v, rs, off, 0, 16); }
DI void st8(__amdgpu_buffer_rsrc_t rs, unsigned off, v2u v) { __builtin_amdgcn_raw_buffer_store_b64(v, rs, off, 0, 16); }
DI bool ok4(v4u v) { return v[0] != SENT && v[1] != SENT && v[2] != SENT && v[3] != SENT; }
DI f32x4 mfma16(bf16x8 a, bf16x8 b, f32x4 c) { return __builtin_amdgcn_mfma_f32_16x16x32_bf16(a, b, c, 0, 0, 0); }
template <int P> DI float dpp(float v) { return __int_as_float(__builtin_amdgcn_mov_dpp(__float_as_int(v), P, 0xf, 0xf, true)); }
DI float sum16(float v) { auto r = __builtin_amdgcn_permlane16_swap(__float_as_uint(v), __float_as_uint(v), false, false); return __uint_as_float(r[0]) + __uint_as_float(r[1]); }
DI float sum32(float v) { auto r = __builtin_amdgcn_permlane32_swap(__float_as_uint(v), __float_as_uint(v), false, false); return __uint_as_float(r[0]) + __uint_as_float(r[1]); }
DI float max16(float v) { auto r = __builtin_amdgcn_permlane16_swap(__float_as_uint(v), __float_as_uint(v), false, false); return fmaxf(__uint_as_float(r[0]), __uint_as_float(r[1])); }
DI float max32(float v) { auto r = __builtin_amdgcn_permlane32_swap(__float_as_uint(v), __float_as_uint(v), false, false); return fmaxf(__uint_as_float(r[0]), __uint_as_float(r[1])); }
DI float rowsum(float v) { v += dpp<0x128>(v); v += dpp<0x124>(v); v += dpp<0x122>(v); v += dpp<0x121>(v); return v; }
DI float rowmax(float v) { v = fmaxf(v, dpp<0x128>(v)); v = fmaxf(v, dpp<0x124>(v)); v = fmaxf(v, dpp<0x122>(v)); v = fmaxf(v, dpp<0x121>(v)); return v; }
DI float halfsum(float v) { return sum16(rowsum(v)); }
DI float other_half(float v) { auto r = __builtin_amdgcn_permlane32_swap(__float_as_uint(v), __float_as_uint(v), false, false); return __uint_as_float((threadIdx.x & 32) ? r[0] : r[1]); }
DI f32x16 mfma32(bf16x8 a, bf16x8 b, f32x16 c) { return __builtin_amdgcn_mfma_f32_32x32x16_bf16(a, b, c, 0, 0, 0); }
DI bf16x8 trfrag(LAS char* L, unsigned a0, unsigned a1) {
    s16x4 x = __builtin_amdgcn_ds_read_tr16_b64_v4i16((LAS s16x4*)(L + a0));
    s16x4 y = __builtin_amdgcn_ds_read_tr16_b64_v4i16((LAS s16x4*)(L + a1));
    return __builtin_shufflevector(x, y, 0, 1, 2, 3, 4, 5, 6, 7);
}
DI bf16x8 asb(v4u v) { return __builtin_bit_cast(bf16x8, v); }
DI f32x4 fold(f32x4 d) { return f32x4{d[0] + dpp<0xEE>(d[0]), d[1] + dpp<0xEE>(d[1]), d[2] + dpp<0xEE>(d[2]), d[3] + dpp<0xEE>(d[3])}; }
DI v4u nos4(f32x4 d) { return v4u{nosent(d[0]), nosent(d[1]), nosent(d[2]), nosent(d[3])}; }
DI v4u split8(const float* n, bool lo_part) { v4u o;
#pragma unroll
    for (int j = 0; j < 4; ++j) { unsigned hi = pk2(n[2 * j], n[2 * j + 1]); o[j] = lo_part ? pk2(n[2 * j] - lo_f(hi), n[2 * j + 1] - hi_f(hi)) : hi; }
    return o; }
template <int N> DI void vmw() { asm volatile("s_waitcnt vmcnt(%0)" :: "n"(N) : "memory"); }

#define SB() __builtin_amdgcn_sched_barrier(0)

__global__ __launch_bounds__(NT, 2) void mk(const u16* __restrict__ x, const u16* __restrict__ ckv, const u16* __restrict__ wq, const u16* __restrict__ wuk,
                                           const u16* __restrict__ wuv, const u16* __restrict__ wo, const u16* __restrict__ wg, const u16* __restrict__ wu,
                                           const u16* __restrict__ wd, u16* __restrict__ out, char* __restrict__ ws) {
    extern __shared__ __attribute__((aligned(16))) char smem_[];
    LAS char* L = (LAS char*)smem_;
    const int tid = threadIdx.x, lane = tid & 63, wave = __builtin_amdgcn_readfirstlane(tid >> 6);
    const int g = lane >> 4, l15 = lane & 15, l31 = lane & 31, hf = lane >> 5;
    const int q4 = l15 >> 2, p4 = lane & 3, b1 = lane & 1;
    const int bid = blockIdx.x, xc = bid & 7, jj = bid >> 3;
    const bool isQ = jj < 16;
    const int idx = jj & 15, a = xc * 16 + idx;
    const int hh = xc * 4 + (idx >> 2), rr = idx & 3;
    const int bb = a >> 6, split = (a & 63) + (isQ ? 64 : 0);
    const __amdgpu_buffer_rsrc_t rs = __builtin_amdgcn_make_buffer_rsrc(ws, 0, 0x10000000, 0x00020000);
    const u16* kvg = ckv + ((size_t)bb * SK + split * 128) * DC;
    const f32x4 z4 = {0.f, 0.f, 0.f, 0.f};
    auto dma_kv = [&](int row) { glds(kvg + (size_t)row * DC + ((lane ^ sw(row)) << 3), (unsigned)row * 1024u, 1); };
    unsigned t0a, t0b;
    { int ra = 8 * g + q4, rb = ra + 4, ch = 2 * wave + (p4 >> 1); t0a = ra * 256 + ((ch ^ sw(ra)) << 4) + 8 * (p4 & 1); t0b = rb * 256 + ((ch ^ sw(rb)) << 4) + 8 * (p4 & 1); }
    unsigned tag0, tag1;
    bf16x8 fuv[4];
    constexpr unsigned XRAW = AUX + 20480, EPL = AUX + 24576, QIMG = AUX + 26624, WQI = 88 * 1024, WKI = 96 * 1024;
    if (!isQ) {
        const int nk = rr < 2 ? 5 : 4, row0 = rr < 2 ? 160 * rr : 64 + 128 * rr;
        v4u xf[5], xs[3]; v2u ep;
        if (wave == 7) {
            glds(x + lane * 8, XRAW, 0); glds(x + 512 + lane * 8, XRAW + 1024, 0); glds(x + 1024 + (lane & 15) * 8, XRAW + 2048, 0);
            glds(ws + WS_EPOCH + lane * 16, EPL, 2);
        }
        for (int n = wave; n < 8 * nk; n += 8) { int row = 4 * n + g; glds(wq + (size_t)(row0 + row) * 4096 + hh * 128 + ((l15 ^ sw(row)) << 3), WQI + n * 1024, 1); }
#pragma unroll
        for (int i = 0; i < NA1 / 8; ++i) dma_kv(wave + 8 * i);
        unsigned e1w = WS_E1 + ((hh * 4 + rr) * 2 + b1) * 1024 + (16 * wave + 4 * g + (l15 & 2)) * 8;
        unsigned wa = WQI + t0a, wb = WQI + t0b;
        pin(e1w); pin(wa); pin(wb);
        if (wave == 7) vmw<4 + NA1 / 8>();
        bar();
        ep = *(LAS v2u*)(L + EPL);
        xs[0] = *(LAS v4u*)(L + XRAW + hf * 1152 + l31 * 16); xs[1] = *(LAS v4u*)(L + XRAW + hf * 1152 + 512 + l31 * 16); xs[2] = *(LAS v4u*)(L + XRAW + hf * 1152 + 1024 + (l31 & 7) * 16);
#pragma unroll
        for (int ks = 0; ks < 5; ++ks) xf[ks] = *(LAS v4u*)(L + XRAW + b1 * 1152 + (row0 + 32 * (ks < nk ? ks : 0) + 8 * g) * 2);
        { u64 t = (((u64)ep[1] << 32) | ep[0]) + 1ull; tag0 = (unsigned)t; tag1 = (unsigned)(t >> 32); }
        float mu, rsd;
        {
            const float ok2 = l31 < 8 ? 1.f : 0.f;
            float v[24]; float s = 0.f;
#pragma unroll
            for (int i = 0; i < 3; ++i)
#pragma unroll
                for (int j = 0; j < 4; ++j) { v[8 * i + 2 * j] = lo_f(xs[i][j]); v[8 * i + 2 * j + 1] = hi_f(xs[i][j]); }
            float s2 = 0.f;
#pragma unroll
            for (int i = 0; i < 16; ++i) s += v[i];
#pragma unroll
            for (int i = 16; i < 24; ++i) s2 += v[i];
            mu = halfsum(s + ok2 * s2) * (1.f / 576.f); float q = 0.f, q2 = 0.f;
#pragma unroll
            for (int i = 0; i < 16; ++i) { float d = v[i] - mu; q += d * d; }
#pragma unroll
            for (int i = 16; i < 24; ++i) { float d = v[i] - mu; q2 += d * d; }
            rsd = __builtin_amdgcn_rcpf(rb(__builtin_amdgcn_sqrtf(rb(rb(halfsum(q + ok2 * q2) * (1.f / 576.f)) + 1e-5f)))); mu = rb(mu);
            float mo = other_half(mu), ro = other_half(rsd);
            if (b1 != hf) { mu = mo; rsd = ro; }
#pragma unroll
            for (int ks = 0; ks < 5; ++ks) {
#pragma unroll
                for (int j = 0; j < 4; ++j) { unsigned dd = pk2(lo_f(xf[ks][j]) - mu, hi_f(xf[ks][j]) - mu); xf[ks][j] = pk2(lo_f(dd) * rsd, hi_f(dd) * rsd); }
                pin(xf[ks]); }
        }
        unsigned t40lo = tag0 << 24, t40hi = (tag0 >> 8) | (tag1 << 24);
        pin(t40lo); pin(t40hi);
        vmw<NA1 / 8>();
        bar();
        { f32x4 d = z4;
#pragma unroll
          for (int ks = 0; ks < 4; ++ks) d = mfma16(trfrag(L, wa + ks * 8192, wb + ks * 8192), asb(xf[ks]), d);
          if (nk == 5) d = mfma16(trfrag(L, wa + 32768, wb + 32768), asb(xf[4]), d);
          float e0 = dpp<0x44>(d[2]), e1 = dpp<0x44>(d[3]);
          if (l15 & 2) { d[0] = e0; d[1] = e1; }
          if (l15 < 4) st16(rs, e1w, v4u{((__float_as_uint(d[0]) + 0x80u) >> 8) | t40lo, t40hi, ((__float_as_uint(d[1]) + 0x80u) >> 8) | t40lo, t40hi}); }
        bar();
        for (int n = wave; n < 32; n += 8) { int e = 4 * n + g; int c = 32 * (e & 3) + 16 * ((e >> 2) & 1) + (e >> 3); glds(wuv + ((size_t)(128 * rr + c) * 32 + hh) * 128 + ((l15 ^ sw(e)) << 3), AUX + n * 1024, 1); }
#pragma unroll
        for (int i = NA1 / 8; i < 16; ++i) { dma_kv(wave + 8 * i); if (i >= G1_F2) vmw<G1_F2>(); }
        bar();
#pragma unroll
        for (int ks = 0; ks < 4; ++ks) { fuv[ks] = trfrag(L, AUX + t0a + ks * 8192 - 65536 + 65536, AUX + t0b + ks * 8192); pin(fuv[ks]); }
        vm0();
    } else {
        if (wave == 7) glds(ws + WS_EPOCH + lane * 16, EPL, 2);
        if (wave == 6) { const char* tp = lane < 8 ? ws + WS_FLAG + lane * 1024 + (a >> 4) * 128 : ws + WS_QA + (((lane & 1) * 32 + hh) * 512 + 128 * rr + ((lane >> 1) & 1) * 64) * 2; glds(tp, EPL + 3072, 2); }
        for (int n = wave; n < 32; n += 8) { int row = 4 * n + g; glds(wuk + ((size_t)(128 * rr + row) * 32 + hh) * 128 + ((l15 ^ sw(row)) << 3), WKI + n * 1024, 1); }
        { v4u s4 = {SENT, SENT, SENT, SENT};
          st16(rs, WS_CLR + a * CLR_PER + wave * 1024 + lane * 16, s4);
          st16(rs, WS_CLR + a * CLR_PER + 8192 + (wave < 5 ? wave : 0) * 1024 + lane * 16, s4); }
#pragma unroll
        for (int ks = 0; ks < 4; ++ks) fuv[ks] = bf16x8{0, 0, 0, 0, 0, 0, 0, 0};
#pragma unroll
        for (int i = 0; i < NA2 / 8; ++i) dma_kv(wave + 8 * i);
        unsigned e1r = WS_E1 + ((hh * 4 + g) * 2 + (l15 >> 3)) * 1024 + (16 * wave + 2 * (lane & 7)) * 8;
        unsigned qw = QIMG + (l15 >> 3) * 272 + (16 * wave + 2 * (lane & 7)) * 2;
        unsigned ka0, ka1, qr, qaw;
        { const int c0 = 32 * (wave & 3) + 8 * q4 + (l15 & 3), c1 = c0 + 4; ka0 = WKI + c0 * 256 + ((g ^ sw(c0)) << 4); ka1 = WKI + c1 * 256 + ((g ^ sw(c1)) << 4);
          qr = QIMG + b1 * 272 + 16 * g; qaw = WS_QA + ((b1 * 32 + hh) * 512 + 128 * rr + 32 * (wave & 3) + 8 * g) * 2; }
        pin(e1r); pin(qw); pin(ka0); pin(ka1); pin(qr); pin(qaw);
        vm0();
        bar();
        { v2u ep = *(LAS v2u*)(L + EPL); u64 t = (((u64)ep[1] << 32) | ep[0]) + 1ull; tag0 = (unsigned)t; tag1 = (unsigned)(t >> 32); }
        const unsigned t40lo = tag0 << 24, t40hi = (tag0 >> 8) | (tag1 << 24);
        { v4u v; bool okv = false;
          for (int sp = 0; sp < SPIN_CAP; ++sp) { asm volatile("" ::: "memory");
              if (!okv) v = ld16(rs, e1r);
              okv = (v[1] == t40hi) & (v[3] == t40hi) & ((v[0] & 0xFF000000u) == t40lo) & ((v[2] & 0xFF000000u) == t40lo);
              if (__all(okv)) break; if (E1_SLEEP) __builtin_amdgcn_s_sleep(E1_SLEEP); }
          float q0 = sum32(sum16(__uint_as_float(v[0] << 8))), q1 = sum32(sum16(__uint_as_float(v[2] << 8)));
          if (lane < 16) *(LAS unsigned*)(L + qw) = pk2(q0, q1);
        }
        bar();
        if (wave < 4) {
            f32x4 d0 = z4, d1 = z4;
#pragma unroll
            for (int ks = 0; ks < 4; ++ks) { bf16x8 bq = *(LAS bf16x8*)(L + qr + 64 * ks);
                d0 = mfma16(*(LAS bf16x8*)(L + (ka0 ^ (ks << 6))), bq, d0); d1 = mfma16(*(LAS bf16x8*)(L + (ka1 ^ (ks << 6))), bq, d1); }
            if (l15 < 2) st16(rs, qaw, v4u{pk2(d0[0], d0[1]), pk2(d0[2], d0[3]), pk2(d1[0], d1[1]), pk2(d1[2], d1[3])});
            vm0();
        }
        bar();
        if (wave == 0 && lane < 8) st8(rs, WS_FLAG + lane * 1024 + a * 8, v2u{tag0, tag1});
        if (wave >= 4) for (int r = NA2 + wave - 4; r < NA2 + NB2; r += 4) dma_kv(r);
    }
    const int NAr = isQ ? NA2 : 128, NBr = isQ ? NB2 : 0;
    unsigned a8[8];
#pragma unroll
    for (int j = 0; j < 8; ++j) { a8[j] = AUX + l31 * 1024 + (((2 * j + hf) ^ sw(l31)) << 4); pin(a8[j]); }
    if (wave == 0) {
        for (int sp = 0; sp < SPIN_CAP; ++sp) { asm volatile("" ::: "memory"); v4u f = ld16(rs, WS_FLAG + xc * 1024 + lane * 16);
            if (__all(f[0] == tag0 && f[1] == tag1 && f[2] == tag0 && f[3] == tag1)) break; }
    }
    bar();
    if (wave < 4) for (int r = wave; r < 32; r += 4) glds(ws + WS_QA + (bb * 32 + r) * 1024 + ((lane ^ sw(r)) << 4), AUX + r * 1024, 2);
    bar();
    if (wave < 4) vm0(); else for (int r = NAr + NBr + wave - 4; r < NAr + NBr + NC_ROWS && r < 128; r += 4) dma_kv(r);
    bar();
    if (wave >= 4) for (int r = NAr + NBr + NC_ROWS + wave - 4; r < 128; r += 4) dma_kv(r);
    constexpr unsigned WOI = 32768, PIM = 69632, SCR = 135168, WDI_ = 92160;
    const int af_ = FFN_MAP ? idx * 8 + xc : a;
    const int u0_ = (9 * af_) >> 1, nu_ = ((9 * (af_ + 1)) >> 1) - u0_;
    auto dma_gu = [&](int n) { int q = n * 64 + lane; int row = q / 10, k = q - row * 10; int ku = k >= 5 ? k - 5 : k; if (ku >= nu_) ku = nu_ - 1;
        glds((k >= 5 ? wu : wg) + (size_t)row * FF + (u0_ + ku) * 8, n * 1024, FFN_AUX); };
    auto dma_cw = [&](int n) { glds(wo + (size_t)(hh * 128 + 32 * rr) * MD + n * 512 + lane * 8, WOI + n * 1024, 1); };
    {
        LAS float* mx = (LAS float*)(L + AUX);
        LAS float* lsum = (LAS float*)(L + AUX + 512);
        LAS char* pim = L + AUX + 1024;
        if (wave < 4) {
            bf16x8 qf[32], kf[16];
#pragma unroll
            for (int s = 0; s < 32; ++s) qf[s] = *(LAS bf16x8*)(L + a8[s & 7] + (s >> 3) * 256);
#pragma unroll
            for (int j = 0; j < 8; ++j) { a8[j] += (unsigned)(wave * 32768) - AUX; pin(a8[j]); }
            unsigned pw = AUX + 1024 + l31 * 272 + (32 * wave + 4 * hf) * 2; pin(pw);
            vm0();
            bar();
            f32x16 s;
#pragma unroll
            for (int i = 0; i < 16; ++i) s[i] = 0.f;
#pragma unroll
            for (int st = 0; st < 16; ++st) kf[st] = *(LAS bf16x8*)(L + a8[st & 7] + (st >> 3) * 256);
            SB();
#pragma unroll
            for (int st = 0; st < 16; ++st) { s = mfma32(kf[st], qf[st], s); SB(); kf[st] = *(LAS bf16x8*)(L + a8[st & 7] + (2 + (st >> 3)) * 256); SB(); }
#pragma unroll
            for (int st = 0; st < 16; ++st) s = mfma32(kf[st], qf[16 + st], s);
#pragma unroll
            for (int i = 0; i < 16; i += 2) { unsigned r1 = pk2(s[i], s[i + 1]); unsigned r2 = pk2(lo_f(r1) * 0.08838834764831845f, hi_f(r1) * 0.08838834764831845f);
                s[i] = lo_f(r2) * 1.4426950408889634f; s[i + 1] = hi_f(r2) * 1.4426950408889634f; }
            float m = s[0];
#pragma unroll
            for (int i = 1; i < 16; ++i) m = fmaxf(m, s[i]);
            m = max32(m);
            if (lane < 32) mx[wave * 32 + lane] = m;
            bar();
            const float M = fmaxf(fmaxf(mx[l31], mx[32 + l31]), fmaxf(mx[64 + l31], mx[96 + l31]));
            float ls = 0.f;
#pragma unroll
            for (int gq = 0; gq < 4; ++gq) { float e0 = ex2(s[4 * gq] - M), e1 = ex2(s[4 * gq + 1] - M), e2 = ex2(s[4 * gq + 2] - M), e3 = ex2(s[4 * gq + 3] - M);
                *(LAS v2u*)(L + pw + 16 * gq) = v2u{pk2(e0, e1), pk2(e2, e3)}; ls += (e0 + e1) + (e2 + e3); }
            ls = sum32(ls);
            if (lane < 32) lsum[wave * 32 + lane] = ls;
            bar();
            if (isQ) { for (int n = wave; n < NF_EARLY; n += 4) dma_gu(n); } else { for (int n = wave; n < NW_EARLY; n += 4) dma_cw(n); }
            bar();
        } else {
            const int cq = wave - 4, blk = g & 1;
            unsigned va[4], vb[4];
#pragma unroll
            for (int t = 0; t < 4; ++t) { const int ra = 8 * hf + q4, rb = ra + 4; int ch = 16 * cq + 4 * t + 2 * blk + (p4 >> 1);
                va[t] = ra * 1024 + ((ch ^ sw(ra)) << 4) + 8 * (p4 & 1); vb[t] = rb * 1024 + ((ch ^ sw(rb)) << 4) + 8 * (p4 & 1); pin(va[t]); pin(vb[t]); }
            unsigned pr = AUX + 1024 + l31 * 272 + 16 * hf, po = WS_PO + ((((bb * 32 + 4 * hf + blk) * 4 + cq) * 128 + split) * 16 + l15) * 16; pin(pr); pin(po);
            vm0();
            bar();
            bf16x8 vf[8][4];
#pragma unroll
            for (int ks = 0; ks < 4; ++ks)
#pragma unroll
                for (int t = 0; t < 4; ++t) vf[ks][t] = trfrag(L, va[t] + ks * 16384, vb[t] + ks * 16384);
#pragma unroll
            for (int t = 0; t < 4; ++t) { va[t] += 65536; vb[t] += 65536; }
#pragma unroll
            for (int ks = 0; ks < VPRE; ++ks)
#pragma unroll
                for (int t = 0; t < 4; ++t) vf[4 + ks][t] = trfrag(L, va[t] + ks * 16384, vb[t] + ks * 16384);
            bar();
#pragma unroll
            for (int ks = VPRE; ks < 4; ++ks)
#pragma unroll
                for (int t = 0; t < 4; ++t) vf[4 + ks][t] = trfrag(L, va[t] + ks * 16384, vb[t] + ks * 16384);
            bar();
            f32x16 o[4];
#pragma unroll
            for (int t = 0; t < 4; ++t)
#pragma unroll
                for (int i = 0; i < 16; ++i) o[t][i] = 0.f;
            bf16x8 pa[8];
#pragma unroll
            for (int ks = 0; ks < 8; ++ks) pa[ks] = *(LAS bf16x8*)(L + pr + 32 * ks);
#pragma unroll
            for (int ks = 0; ks < 8; ++ks)
#pragma unroll
                for (int t = 0; t < 4; ++t) o[t] = mfma32(pa[ks], vf[ks][t], o[t]);
#pragma unroll
            for (int r = 0; r < 16; r += 2) {
                auto x0 = __builtin_amdgcn_permlane16_swap(pk2(o[0][r], o[1][r]), pk2(o[0][r + 1], o[1][r + 1]), false, false);
                auto x1 = __builtin_amdgcn_permlane16_swap(pk2(o[2][r], o[3][r]), pk2(o[2][r + 1], o[3][r + 1]), false, false);
                st16(rs, po + ((r & 3) + 8 * (r >> 2)) * 131072, v4u{x0[0], x1[0], x0[1], x1[1]}); }
            v2u sv = {nosent(fmaxf(fmaxf(mx[l31], mx[32 + l31]), fmaxf(mx[64 + l31], mx[96 + l31]))), nosent((lsum[l31] + lsum[32 + l31]) + (lsum[64 + l31] + lsum[96 + l31]))};
            unsigned so = WS_ST + (((bb * 32 + l31) * 4 + cq) * 128 + split) * 8; pin(sv); pin(so);
            if (!(NODRAIN && isQ)) vm0();
            if (lane < 32) st8(rs, so, sv);
            bar();
        }
    }
    if (isQ == (SWAP != 0)) {
        for (int n = NW_EARLY + wave; n < 36; n += 8) dma_cw(n);
        vm0();
        bar();
        bf16x8 fo[5];
#pragma unroll
        for (int i = 0; i < 5; ++i) { int n = wave + 8 * i; if (n > 35) n = 35; int ra = 8 * g + q4;
            fo[i] = trfrag(L, WOI + ra * 1152 + n * 32 + 8 * p4, WOI + (ra + 4) * 1152 + n * 32 + 8 * p4); pin(fo[i]); }
        constexpr unsigned WIMG = SCR, LINV = SCR + 1280, AIMG = SCR + 1536, AIMG2 = SCR + 2816, R3 = SCR + 3328;
        unsigned pa_ = PIM + t0a, pb_ = PIM + t0b, wr = WIMG + b1 * 272 + 16 * g, aw = AIMG + b1 * 272 + (16 * wave + 4 * g) * 2, ar = AIMG + b1 * 272 + 16 * g;
        unsigned e4w = WS_E4 + ((((hh * 4 + (wave >> 1)) * 4 + rr) * 2 + b1) * 32 + 16 * (wave & 1) + 4 * g) * 4, e4r = WS_E4 + (hh * 4 + rr) * 1024 + lane * 16;
        unsigned a2w = AIMG2 + (l15 >> 3) * 80 + (lane & 7) * 8, a2r = AIMG2 + b1 * 80 + g * 16;
        unsigned e5w = WS_E5 + (((b1 * 18 + (wave >> 1)) * 128 + a) * 32 + 16 * (wave & 1) + 4 * g) * 4;
        pin(pa_); pin(pb_); pin(wr); pin(aw); pin(ar); pin(e4w); pin(e4r); pin(a2w); pin(a2r); pin(e5w);
        v2u sv[2];
        const char* src[4];
        if (tid == 0) *(LAS unsigned*)(L + LINV + 16) = 0u;
        {
#pragma unroll
            for (int k = 0; k < 4; ++k) { int n = wave + 8 * k; int b = n >> 4, row = 64 + 4 * (n & 15) + g; src[k] = ws + WS_PO + ((b * 32 + hh) * 4 + rr) * 32768 + row * 256 + ((l15 ^ sw(row)) << 4); pin(src[k]); }
            if (wave < 2) { const unsigned so_ = WS_ST + (((wave * 32 + hh) * 4 + rr) * 128 + lane) * 8;
                for (int sp = 0; sp < SPIN_CAP; ++sp) { asm volatile("" ::: "memory"); sv[0] = ld8(rs, so_); sv[1] = ld8(rs, so_ + 512); if (__all(sv[0][0] != SENT && sv[0][1] != SENT && sv[1][0] != SENT && sv[1][1] != SENT)) break; }
            }
            bar();
#pragma unroll
            for (int k = 0; k < 4; ++k) { int n = wave + 8 * k; glds(src[k] - 16384, PIM + (n >> 4) * 32768 + (n & 15) * 1024, 2); }
#pragma unroll
            for (int k = 0; k < 4; ++k) { int n = wave + 8 * k; glds(src[k], PIM + (n >> 4) * 32768 + (16 + (n & 15)) * 1024, 2); }
        }
        if (wave < 2) { float m0 = __uint_as_float(sv[0][0]), m1 = __uint_as_float(sv[1][0]);
            float M = max32(max16(rowmax(fmaxf(m0, m1))));
            unsigned wp = pk2(ex2(m0 - M), ex2(m1 - M));
            float Ls = sum32(halfsum(lo_f(wp) * __uint_as_float(sv[0][1]) + hi_f(wp) * __uint_as_float(sv[1][1])));
            *(LAS u16*)(L + WIMG + wave * 272 + lane * 2) = (u16)wp; *(LAS u16*)(L + WIMG + wave * 272 + 128 + lane * 2) = (u16)(wp >> 16);
            if (lane == 0) *(LAS float*)(L + LINV + wave * 4) = __builtin_amdgcn_rcpf(Ls); }
        for (unsigned it = 1;; ++it) {
            vm0();
            bar();
            f32x4 d0 = z4, d1 = z4;
            typedef unsigned short us2 __attribute__((ext_vector_type(2)));
            us2 mxu = {0, 0};
#pragma unroll
            for (int ks = 0; ks < 4; ++ks) { bf16x8 bw = *(LAS bf16x8*)(L + wr + 64 * ks);
                bf16x8 f0 = trfrag(L, pa_ + ks * 8192, pb_ + ks * 8192), f1 = trfrag(L, pa_ + 32768 + ks * 8192, pb_ + 32768 + ks * 8192);
                d0 = mfma16(f0, bw, d0); d1 = mfma16(f1, bw, d1);
                if (NODRAIN && ks >= 2) { v4u u0 = __builtin_bit_cast(v4u, f0), u1 = __builtin_bit_cast(v4u, f1);
#pragma unroll
                    for (int j = 0; j < 4; ++j) { mxu = __builtin_elementwise_max(mxu, __builtin_bit_cast(us2, u0[j])); mxu = __builtin_elementwise_max(mxu, __builtin_bit_cast(us2, u1[j])); } } }
            const float inv = *(LAS float*)(L + LINV + b1 * 4);
            f32x4 r = (b1 ? d1 : d0) * inv;
            if (l15 < 2) *(LAS v2u*)(L + aw) = v2u{pk2(r[0], r[1]), pk2(r[2], r[3])};
            if (NODRAIN && (mxu[0] == 0xFFFFu || mxu[1] == 0xFFFFu)) *(LAS unsigned*)(L + LINV + 16) = it;
            bar();
            if (!NODRAIN) break;
            if (__builtin_amdgcn_readfirstlane(*(LAS unsigned*)(L + LINV + 16)) != it || it > 2000u) break;
#pragma unroll
            for (int k = 0; k < 4; ++k) { int n = wave + 8 * k; glds(src[k], PIM + (n >> 4) * 32768 + (16 + (n & 15)) * 1024, 2); }
        }
        { f32x4 d = z4;
#pragma unroll
          for (int ks = 0; ks < 4; ++ks) d = mfma16(fuv[ks], *(LAS bf16x8*)(L + ar + 64 * ks), d);
          if (l15 < 2) st16(rs, e4w, nos4(d)); }
        if (wave == 0) { v4u v;
            for (int sp = 0; sp < SPIN_CAP; ++sp) { asm volatile("" ::: "memory"); v = ld16(rs, e4r); if (__all(ok4(v))) break; }
            float c[4];
#pragma unroll
            for (int j = 0; j < 4; ++j) c[j] = sum32(sum16(__uint_as_float(v[j])));
            if (lane < 16) *(LAS v2u*)(L + a2w) = v2u{pk2(c[0], c[1]), pk2(c[2], c[3])}; }
        bar();
        { bf16x8 av = *(LAS bf16x8*)(L + a2r);
          f32x4 d[5];
#pragma unroll
          for (int i = 0; i < 5; ++i) d[i] = mfma16(fo[i], av, z4);
          if (l15 < 2) {
#pragma unroll
              for (int i = 0; i < 4; ++i) st16(rs, e5w + i * 65536, nos4(d[i]));
              if (wave < 4) st16(rs, e5w + 4 * 65536, nos4(d[4])); } }
        const int rho = xc + 8 * idx;
        if (rho < 36) {
            LAS float* r3 = (LAS float*)(L + R3);
            f32x4 hv;
            const v2u xv = *(const v2u*)(x + rho * 32 + (lane & 7) * 4);
#pragma unroll
            for (int ph = 0; ph < 2; ++ph) {
                const unsigned src = (ph ? WS_E7 : WS_E5) + rho * 16384 + tid * 16;
                v4u v0, v1;
                bool k0 = false, k1 = false;
                for (int sp = 0; sp < SPIN_CAP; ++sp) { asm volatile("" ::: "memory"); if (!k0) v0 = ld16(rs, src); if (!k1) v1 = ld16(rs, src + 8192); k0 = ok4(v0); k1 = ok4(v1); if (__all(k0 && k1)) break; if (R_SLEEP) __builtin_amdgcn_s_sleep(R_SLEEP); }
                f32x4 s;
#pragma unroll
                for (int j = 0; j < 4; ++j) { float t = __uint_as_float(v0[j]) + __uint_as_float(v1[j]); t += dpp<0x128>(t); s[j] = sum32(sum16(t)); }
                if (lane < 8) *(LAS f32x4*)(r3 + wave * 32 + lane * 4) = s;
                bar();
                if (wave == 0) { const int q = lane & 7; f32x4 t = z4;
#pragma unroll
                    for (int w = 0; w < 8; ++w) t += *(LAS f32x4*)(r3 + w * 32 + q * 4);
                    if (ph == 0) {
                        { unsigned a0 = pk2(t[0], t[1]), a1 = pk2(t[2], t[3]); hv = f32x4{lo_f(a0) + lo_f(xv[0]), hi_f(a0) + hi_f(xv[0]), lo_f(a1) + lo_f(xv[1]), hi_f(a1) + hi_f(xv[1])}; }
                        unsigned h0 = pk2(hv[0], hv[1]), h1 = pk2(hv[2], hv[3]);
                        hv = f32x4{lo_f(h0), hi_f(h0), lo_f(h1), hi_f(h1)};
                        st8(rs, WS_H + (lane >> 3) * 8192 + rho * 64 + q * 8, v2u{__builtin_elementwise_min(h0, 0xFFFFFFFEu), __builtin_elementwise_min(h1, 0xFFFFFFFEu)});
                        f32x4 r = {lo_f(h0), hi_f(h0), lo_f(h1), hi_f(h1)};
                        float sm = (r[0] + r[1]) + (r[2] + r[3]); sm += dpp<0xB1>(sm); sm += dpp<0x4E>(sm); sm += dpp<0x141>(sm);
                        r -= sm * (1.f / 32.f);
                        float m2 = (r[0] * r[0] + r[1] * r[1]) + (r[2] * r[2] + r[3] * r[3]); m2 += dpp<0xB1>(m2); m2 += dpp<0x4E>(m2); m2 += dpp<0x141>(m2);
                        if (q == 0) st8(rs, WS_H + (lane >> 3) * 8192 + 4096 + rho * 8, v2u{nosent(sm), nosent(m2)});
                        if (rho == 0 && lane == 0) st8(rs, WS_EPOCH, v2u{tag0, tag1});
                    } else if (lane < 8) { unsigned a0 = pk2(t[0], t[1]), a1 = pk2(t[2], t[3]); *(v2u*)(out + rho * 32 + q * 4) = v2u{pk2(hv[0] + lo_f(a0), hv[1] + hi_f(a0)), pk2(hv[2] + lo_f(a1), hv[3] + hi_f(a1))}; } }
                bar();
            }
        }
    } else {
        const int u0 = u0_, nu = nu_;
        constexpr unsigned WDI = 92160;
        if (FFN_SLEEP) __builtin_amdgcn_s_sleep(FFN_SLEEP);
        {
            for (int n = NF_EARLY + wave; n < 90; n += 8) dma_gu(n);
            for (int n = wave; n < 9 * nu; n += 8) glds(wd + (size_t)u0 * 8 * MD + n * 512 + lane * 8, WDI + n * 1024, 1);
        }
        vm0();
        bar();
        bf16x8 fg[3][5], fd[5][2];
#pragma unroll
        for (int kk = 0; kk < 3; ++kk) { int ks = wave + 8 * kk; if (ks > 17) ks = 17; int ra = 32 * ks + 8 * g + q4;
#pragma unroll
            for (int t = 0; t < 5; ++t) { fg[kk][t] = trfrag(L, ra * 160 + t * 32 + 8 * p4, (ra + 4) * 160 + t * 32 + 8 * p4); pin(fg[kk][t]); } }
#pragma unroll
        for (int i = 0; i < 5; ++i) { int n = wave + 8 * i; if (n > 35) n = 35; int ra = 8 * g + q4;
            fd[i][0] = trfrag(L, WDI + ra * 1152 + n * 32 + 8 * p4, WDI + (ra + 4) * 1152 + n * 32 + 8 * p4);
            int rc = 32 + q4;
            bf16x8 f1 = trfrag(L, WDI + rc * 1152 + n * 32 + 8 * p4, WDI + (rc + 4) * 1152 + n * 32 + 8 * p4);
            if (g != 0 || nu < 5) f1 = bf16x8{0, 0, 0, 0, 0, 0, 0, 0};
            fd[i][1] = f1; pin(fd[i][0]); pin(fd[i][1]); }
        constexpr unsigned RED2 = 144384, AIMG = 149504, STS = 150528;
        unsigned hsrc = WS_H + xc * 8192 + b1 * 1152 + (32 * wave + 8 * g) * 2, ssrc = WS_H + xc * 8192 + 4096 + ((wave & 1) * 18 + (l31 < 18 ? l31 : 0)) * 8;
        unsigned r2w = RED2 + ((wave * 2 + b1) * 80 + 4 * g) * 4, r2r = RED2 + ((wave & 1) * 80 + (lane < 40 ? lane : 0)) * 4, acw = AIMG + (wave & 1) * 144 + lane * 2, acr = AIMG + b1 * 144 + g * 16;
        unsigned e7w = WS_E7 + (((b1 * 18 + (wave >> 1)) * 128 + a) * 32 + 16 * (wave & 1) + 4 * g) * 4;
        float amask = lane < 8 * nu ? 1.f : 0.f, ok18 = l31 < 18 ? 1.f : 0.f;
        float csg = 0.f, csu = 0.f;
        {
            const bf16x8 ones = asb(v4u{0x3F803F80u, 0x3F803F80u, 0x3F803F80u, 0x3F803F80u});
            f32x4 d[5];
#pragma unroll
            for (int t = 0; t < 5; ++t) { d[t] = mfma16(fg[1][t], ones, mfma16(fg[0][t], ones, z4)); if (wave < 2) d[t] = mfma16(fg[2][t], ones, d[t]); }
            if (l15 < 2) {
#pragma unroll
                for (int t = 0; t < 5; ++t) *(LAS f32x4*)(L + r2w + 64 * t) = d[t]; }
            bar();
#pragma unroll
            for (int w = 0; w < 8; ++w) { csg += *(LAS float*)(L + r2r + w * 640); csu += *(LAS float*)(L + r2r + w * 640 + 160); }
            bar();
        }
        pin(hsrc); pin(ssrc); pin(r2w); pin(r2r); pin(acw); pin(acr); pin(e7w); pin(amask); pin(ok18); pin(csg); pin(csu);
        { v4u hv[3]; v2u sv;
          for (int sp = 0; sp < SPIN_CAP; ++sp) { asm volatile("" ::: "memory");
              hv[0] = ld16(rs, hsrc); hv[1] = ld16(rs, hsrc + 512); bool ok = ok4(hv[0]) && ok4(hv[1]);
              if (wave < 2) { hv[2] = ld16(rs, hsrc + 1024); ok = ok && ok4(hv[2]); }
              if (wave >= 6) { sv = ld8(rs, ssrc); ok = ok && sv[0] != SENT && sv[1] != SENT; }
              if (__all(ok)) break; __builtin_amdgcn_s_sleep(H_SLEEP); }
          f32x4 d[5];
#pragma unroll
          for (int t = 0; t < 5; ++t) d[t] = mfma16(fg[0][t], asb(hv[0]), z4);
#pragma unroll
          for (int t = 0; t < 5; ++t) d[t] = mfma16(fg[1][t], asb(hv[1]), d[t]);
          if (wave < 2) {
#pragma unroll
              for (int t = 0; t < 5; ++t) d[t] = mfma16(fg[2][t], asb(hv[2]), d[t]); }
          if (wave >= 6) { float sm = __uint_as_float(sv[0]) * ok18; float mu = halfsum(sm) * (1.f / 576.f); float dm = sm * (1.f / 32.f) - mu;
              float var = halfsum((__uint_as_float(sv[1]) + 32.f * dm * dm) * ok18) * (1.f / 576.f);
              if (lane == 0) *(LAS v2u*)(L + STS + (wave & 1) * 8) = v2u{__float_as_uint(rb(mu)), __float_as_uint(__builtin_amdgcn_rcpf(rb(__builtin_amdgcn_sqrtf(rb(rb(var) + 1e-5f)))))}; }
          if (l15 < 2) {
#pragma unroll
              for (int t = 0; t < 5; ++t) *(LAS f32x4*)(L + r2w + 64 * t) = d[t]; } }
        bar();
        if (wave < 2) {
            float gs = 0.f, us = 0.f;
            const v2u st = *(LAS v2u*)(L + STS + wave * 8); const float mu = __uint_as_float(st[0]), rsd = __uint_as_float(st[1]);
#pragma unroll
            for (int w = 0; w < 8; ++w) { gs += *(LAS float*)(L + r2r + w * 640); us += *(LAS float*)(L + r2r + w * 640 + 160); }
            gs = rb((gs - mu * csg) * rsd); us = rb((us - mu * csu) * rsd);
            float act = rb(gs * __builtin_amdgcn_rcpf(1.f + __expf(-gs))) * us;
            act = amask != 0.f ? act : 0.f;
            *(LAS u16*)(L + acw) = (u16)pk2(act, 0.f); }
        bar();
        { bf16x8 a0 = *(LAS bf16x8*)(L + acr), a1 = *(LAS bf16x8*)(L + acr + 64);
          f32x4 d[5];
#pragma unroll
          for (int i = 0; i < 5; ++i) d[i] = mfma16(fd[i][0], a0, z4);
#pragma unroll
          for (int i = 0; i < 5; ++i) d[i] = mfma16(fd[i][1], a1, d[i]);
          if (l15 < 2) {
#pragma unroll
              for (int i = 0; i < 4; ++i) st16(rs, e7w + i * 65536, nos4(d[i]));
              if (wave < 4) st16(rs, e7w + 4 * 65536, nos4(d[4])); } }
    }
}

extern "C" void kernel_launch(void* const* d_in, const int* in_sizes, int n_in, void* d_out, int out_size, void* d_ws, size_t ws_size, hipStream_t stream) {
    (void)hipFuncSetAttribute((const void*)mk, hipFuncAttributeMaxDynamicSharedMemorySize, LDS_BYTES);
    mk<<<256, NT, LDS_BYTES, stream>>>((const u16*)d_in[0], (const u16*)d_in[1], (const u16*)d_in[2], (const u16*)d_in[3], (const u16*)d_in[4], (const u16*)d_in[5],
                                       (const u16*)d_in[7], (const u16*)d_in[8], (const u16*)d_in[9], (u16*)d_out, (char*)d_ws);
}
```

```cpp
#include <hip/hip_runtime.h>
#include <hip/hip_bf16.h>
#include <stdint.h>

typedef unsigned short u16;
typedef unsigned long long u64;
typedef short bf16x8 __attribute__((ext_vector_type(8)));
typedef short s16x4 __attribute__((ext_vector_type(4)));
typedef float f32x4 __attribute__((ext_vector_type(4)));
typedef float f32x16 __attribute__((ext_vector_type(16)));
typedef unsigned v4u __attribute__((ext_vector_type(4)));
typedef unsigned v2u __attribute__((ext_vector_type(2)));
#define LAS __attribute__((address_space(3)))
#define GAS __attribute__((address_space(1)))
#define DI __device__ __forceinline__


#define NC_ROWS 32
#define E1_SLEEP 2
#define FFN_SLEEP 0
#define FFN_AUX 0
#define SWAP 0
#define NF_EARLY 16
#define NW_EARLY 0
#define NODRAIN 0
#define H_SLEEP 2
#define R_SLEEP 0
#define VPRE 4
#define FFN_MAP 0
#define KD 12
#define NA1 16
#define NA2 40
#define NB2 40
#define G1_F2 5

constexpr int SK = 16384, MD = 576, DC = 512, FF = 4608;
constexpr int NT = 512;
constexpr int LDS_BYTES = 163840;
constexpr int AUX = 131072;
constexpr unsigned WS_EPOCH = 0;
constexpr unsigned WS_FLAG = 4096;
constexpr unsigned WS_E1 = 32768;
constexpr unsigned WS_QA = WS_E1 + 262144;
constexpr unsigned WS_CLR = WS_QA + 65536;
constexpr unsigned WS_ST = WS_CLR;
constexpr unsigned WS_E4 = WS_ST + 262144;
constexpr unsigned WS_E5 = WS_E4 + 131072;
constexpr unsigned WS_E7 = WS_E5 + 589824;
constexpr unsigned WS_H = WS_E7 + 589824;
constexpr unsigned WS_CLR_END = WS_H + 65536 + 65536;
constexpr unsigned CLR_PER = (WS_CLR_END - WS_CLR) / 128;
static_assert(CLR_PER * 128 == WS_CLR_END - WS_CLR && CLR_PER % 1024 == 0, "clr");
constexpr unsigned WS_PO = 4194304;
constexpr unsigned SENT = 0xFFFFFFFFu;
constexpr int SPIN_CAP = 400000;

DI unsigned pk2(float a, float b) {
    typedef __bf16 bf2 __attribute__((ext_vector_type(2)));
    typedef float f2 __attribute__((ext_vector_type(2)));
    f2 v = {a, b}; bf2 r = __builtin_convertvector(v, bf2); return __builtin_bit_cast(unsigned, r);
}
DI float lo_f(unsigned p) { return __uint_as_float(p << 16); }
DI float hi_f(unsigned p) { return __uint_as_float(p & 0xffff0000u); }
DI unsigned nosent(float v) { return __builtin_elementwise_min(__float_as_uint(v), 0xFFFFFFFEu); }
template <class T> DI void pin(T& v) { asm volatile("" : "+v"(v)); }
DI float rb(float v) { return lo_f(pk2(v, 0.f)); }
DI float ex2(float v) { return __builtin_amdgcn_exp2f(v); }
DI int sw(int row) { return ((row & 3) << 2) | ((row >> 2) & 3); }
DI void bar() { asm volatile("" ::: "memory"); __builtin_amdgcn_s_waitcnt(0xC07F); asm volatile("s_waitcnt lgkmcnt(0)\n\ts_barrier" ::: "memory"); }
DI void glds(const void* g, unsigned lds_, int mode) {
    const unsigned lds = __builtin_amdgcn_readfirstlane(lds_);
    unsigned keep;
    if (mode == 1) asm volatile("s_mov_b32 %0, m0\n\ts_mov_b32 m0, %2\n\ts_nop 0\n\tglobal_load_lds_dwordx4 %1, off nt\n\ts_mov_b32 m0, %0" : "=&s"(keep) : "v"(g), "s"(lds) : "memory");
    else if (mode == 2) asm volatile("s_mov_b32 %0, m0\n\ts_mov_b32 m0, %2\n\ts_nop 0\n\tglobal_load_lds_dwordx4 %1, off sc1\n\ts_mov_b32 m0, %0" : "=&s"(keep) : "v"(g), "s"(lds) : "memory");
    else asm volatile("s_mov_b32 %0, m0\n\ts_mov_b32 m0, %2\n\ts_nop 0\n\tglobal_load_lds_dwordx4 %1, off\n\ts_mov_b32 m0, %0" : "=&s"(keep) : "v"(g), "s"(lds) : "memory");
}
DI void vm0() { asm volatile("s_waitcnt vmcnt(0)" ::: "memory"); }
DI v4u ld16(__amdgpu_buffer_rsrc_t rs, unsigned off) { return __builtin_amdgcn_raw_buffer_load_b128(rs, off, 0, 16); }
DI v2u ld8(__amdgpu_buffer_rsrc_t rs, unsigned off) { return __builtin_amdgcn_raw_buffer_load_b64(rs, off, 0, 16); }
DI void st16(__amdgpu_buffer_rsrc_t rs, unsigned off, v4u v) { __builtin_amdgcn_raw_buffer_store_b128(v, rs, off, 0, 16); }
DI void st8(__amdgpu_buffer_rsrc_t rs, unsigned off, v2u v) { __builtin_amdgcn_raw_buffer_store_b64(v, rs, off, 0, 16); }
DI bool ok4(v4u v) { return v[0] != SENT && v[1] != SENT && v[2] != SENT && v[3] != SENT; }
DI f32x4 mfma16(bf16x8 a, bf16x8 b, f32x4 c) { return __builtin_amdgcn_mfma_f32_16x16x32_bf16(a, b, c, 0, 0, 0); }
template <int P> DI float dpp(float v) { return __int_as_float(__builtin_amdgcn_mov_dpp(__float_as_int(v), P, 0xf, 0xf, true)); }
DI float sum16(float v) { auto r = __builtin_amdgcn_permlane16_swap(__float_as_uint(v), __float_as_uint(v), false, false); return __uint_as_float(r[0]) + __uint_as_float(r[1]); }
DI float sum32(float v) { auto r = __builtin_amdgcn_permlane32_swap(__float_as_uint(v), __float_as_uint(v), false, false); return __uint_as_float(r[0]) + __uint_as_float(r[1]); }
DI float max16(float v) { auto r = __builtin_amdgcn_permlane16_swap(__float_as_uint(v), __float_as_uint(v), false, false); return fmaxf(__uint_as_float(r[0]), __uint_as_float(r[1])); }
DI float max32(float v) { auto r = __builtin_amdgcn_permlane32_swap(__float_as_uint(v), __float_as_uint(v), false, false); return fmaxf(__uint_as_float(r[0]), __uint_as_float(r[1])); }
DI float rowsum(float v) { v += dpp<0x128>(v); v += dpp<0x124>(v); v += dpp<0x122>(v); v += dpp<0x121>(v); return v; }
DI float rowmax(float v) { v = fmaxf(v, dpp<0x128>(v)); v = fmaxf(v, dpp<0x124>(v)); v = fmaxf(v, dpp<0x122>(v)); v = fmaxf(v, dpp<0x121>(v)); return v; }
DI float halfsum(float v) { return sum16(rowsum(v)); }
DI float other_half(float v) { auto r = __builtin_amdgcn_permlane32_swap(__float_as_uint(v), __float_as_uint(v), false, false); return __uint_as_float((threadIdx.x & 32) ? r[0] : r[1]); }
DI f32x16 mfma32(bf16x8 a, bf16x8 b, f32x16 c) { return __builtin_amdgcn_mfma_f32_32x32x16_bf16(a, b, c, 0, 0, 0); }
DI bf16x8 trfrag(LAS char* L, unsigned a0, unsigned a1) {
    s16x4 x = __builtin_amdgcn_ds_read_tr16_b64_v4i16((LAS s16x4*)(L + a0));
    s16x4 y = __builtin_amdgcn_ds_read_tr16_b64_v4i16((LAS s16x4*)(L + a1));
    return __builtin_shufflevector(x, y, 0, 1, 2, 3, 4, 5, 6, 7);
}
DI bf16x8 asb(v4u v) { return __builtin_bit_cast(bf16x8, v); }
DI v4u nos4(f32x4 d) { return v4u{nosent(d[0]), nosent(d[1]), nosent(d[2]), nosent(d[3])}; }
template <int N> DI void vmw() { asm volatile("s_waitcnt vmcnt(%0)" :: "n"(N) : "memory"); }

#define SB() __builtin_amdgcn_sched_barrier(0)

__global__ __launch_bounds__(NT, 2) void mk(const u16* __restrict__ x, const u16* __restrict__ ckv, const u16* __restrict__ wq, const u16* __restrict__ wuk,
                                           const u16* __restrict__ wuv, const u16* __restrict__ wo, const u16* __restrict__ wg, const u16* __restrict__ wu,
                                           const u16* __restrict__ wd, u16* __restrict__ out, char* __restrict__ ws) {
    extern __shared__ __attribute__((aligned(16))) char smem_[];
    LAS char* L = (LAS char*)smem_;
    const int tid = threadIdx.x, lane = tid & 63, wave = __builtin_amdgcn_readfirstlane(tid >> 6);
    const int g = lane >> 4, l15 = lane & 15, l31 = lane & 31, hf = lane >> 5;
    const int q4 = l15 >> 2, p4 = lane & 3, b1 = lane & 1;
    const int bid = blockIdx.x, xc = bid & 7, jj = bid >> 3;
    const bool isQ = jj < 16;
    const int idx = jj & 15, a = xc * 16 + idx;
    const int hh = xc * 4 + (idx >> 2), rr = idx & 3;
    const int bb = a >> 6, split = (a & 63) + (isQ ? 64 : 0);
    const __amdgpu_buffer_rsrc_t rs = __builtin_amdgcn_make_buffer_rsrc(ws, 0, 0x10000000, 0x00020000);
    const u16* kvg = ckv + ((size_t)bb * SK + split * 128) * DC;
    const f32x4 z4 = {0.f, 0.f, 0.f, 0.f};
    auto dma_kv = [&](int row) { glds(kvg + (size_t)row * DC + ((lane ^ sw(row)) << 3), (unsigned)row * 1024u, 1); };
    unsigned t0a, t0b;
    { int ra = 8 * g + q4, rb = ra + 4, ch = 2 * wave + (p4 >> 1); t0a = ra * 256 + ((ch ^ sw(ra)) << 4) + 8 * (p4 & 1); t0b = rb * 256 + ((ch ^ sw(rb)) << 4) + 8 * (p4 & 1); }
    unsigned tag0, tag1;
    bf16x8 fuv[4];
    constexpr unsigned XRAW = AUX + 20480, EPL = AUX + 24576, QIMG = AUX + 26624, WQI = 88 * 1024, WKI = 96 * 1024;
    if (!isQ) {
        const int nk = rr < 2 ? 5 : 4, row0 = rr < 2 ? 160 * rr : 64 + 128 * rr;
        v4u xf[5], xs[3]; v2u ep;
        if (wave == 7) {
            glds(x + lane * 8, XRAW, 0); glds(x + 512 + lane * 8, XRAW + 1024, 0); glds(x + 1024 + (lane & 15) * 8, XRAW + 2048, 0);
            glds(ws + WS_EPOCH + lane * 16, EPL, 2);
        }
        for (int n = wave; n < 8 * nk; n += 8) { int row = 4 * n + g; glds(wq + (size_t)(row0 + row) * 4096 + hh * 128 + ((l15 ^ sw(row)) << 3), WQI + n * 1024, 1); }
#pragma unroll
        for (int i = 0; i < NA1 / 8; ++i) dma_kv(wave + 8 * i);
        unsigned e1w = WS_E1 + ((hh * 4 + rr) * 2 + b1) * 1024 + (16 * wave + 4 * g + (l15 & 2)) * 8;
        unsigned wa = WQI + t0a, wb = WQI + t0b;
        pin(e1w); pin(wa); pin(wb);
        if (wave == 7) vmw<4 + NA1 / 8>();
        bar();
        ep = *(LAS v2u*)(L + EPL);
        xs[0] = *(LAS v4u*)(L + XRAW + hf * 1152 + l31 * 16); xs[1] = *(LAS v4u*)(L + XRAW + hf * 1152 + 512 + l31 * 16); xs[2] = *(LAS v4u*)(L + XRAW + hf * 1152 + 1024 + (l31 & 7) * 16);
        v4u xw = *(LAS v4u*)(L + XRAW + b1 * 1152 + (row0 + 32 * (wave < nk ? wave : 0) + 8 * g) * 2);
        { u64 t = (((u64)ep[1] << 32) | ep[0]) + 1ull; tag0 = (unsigned)t; tag1 = (unsigned)(t >> 32); }
        float mu, rsd;
        {
            const float ok2 = l31 < 8 ? 1.f : 0.f;
            float v[24]; float s = 0.f;
#pragma unroll
            for (int i = 0; i < 3; ++i)
#pragma unroll
                for (int j = 0; j < 4; ++j) { v[8 * i + 2 * j] = lo_f(xs[i][j]); v[8 * i + 2 * j + 1] = hi_f(xs[i][j]); }
            float s2 = 0.f;
#pragma unroll
            for (int i = 0; i < 16; ++i) s += v[i];
#pragma unroll
            for (int i = 16; i < 24; ++i) s2 += v[i];
            mu = halfsum(s + ok2 * s2) * (1.f / 576.f); float q = 0.f, q2 = 0.f;
#pragma unroll
            for (int i = 0; i < 16; ++i) { float d = v[i] - mu; q += d * d; }
#pragma unroll
            for (int i = 16; i < 24; ++i) { float d = v[i] - mu; q2 += d * d; }
            rsd = __builtin_amdgcn_rcpf(rb(__builtin_amdgcn_sqrtf(rb(rb(halfsum(q + ok2 * q2) * (1.f / 576.f)) + 1e-5f)))); mu = rb(mu);
            float mo = other_half(mu), ro = other_half(rsd);
            if (b1 != hf) { mu = mo; rsd = ro; }
#pragma unroll
            for (int j = 0; j < 4; ++j) { unsigned dd = pk2(lo_f(xw[j]) - mu, hi_f(xw[j]) - mu); xw[j] = pk2(lo_f(dd) * rsd, hi_f(dd) * rsd); }
            *(LAS v4u*)(L + AUX + wave * 1024 + lane * 16) = xw;
        }
        unsigned t40lo = tag0 << 24, t40hi = (tag0 >> 8) | (tag1 << 24);
        pin(t40lo); pin(t40hi);
        vmw<NA1 / 8>();
        bar();
        { f32x4 d = z4;
#pragma unroll
          for (int ks = 0; ks < 5; ++ks) xf[ks] = *(LAS v4u*)(L + AUX + ks * 1024 + lane * 16);
#pragma unroll
          for (int ks = 0; ks < 4; ++ks) d = mfma16(trfrag(L, wa + ks * 8192, wb + ks * 8192), asb(xf[ks]), d);
          if (nk == 5) d = mfma16(trfrag(L, wa + 32768, wb + 32768), asb(xf[4]), d);
          float e0 = dpp<0x44>(d[2]), e1 = dpp<0x44>(d[3]);
          if (l15 & 2) { d[0] = e0; d[1] = e1; }
          if (l15 < 4) st16(rs, e1w, v4u{((__float_as_uint(d[0]) + 0x80u) >> 8) | t40lo, t40hi, ((__float_as_uint(d[1]) + 0x80u) >> 8) | t40lo, t40hi}); }
        bar();
        for (int n = wave; n < 32; n += 8) { int e = 4 * n + g; int c = 32 * (e & 3) + 16 * ((e >> 2) & 1) + (e >> 3); glds(wuv + ((size_t)(128 * rr + c) * 32 + hh) * 128 + ((l15 ^ sw(e)) << 3), AUX + n * 1024, 1); }
#pragma unroll
        for (int i = NA1 / 8; i < 16; ++i) { dma_kv(wave + 8 * i); if (i >= G1_F2) vmw<G1_F2>(); }
        bar();
#pragma unroll
        for (int ks = 0; ks < 4; ++ks) { fuv[ks] = trfrag(L, AUX + t0a + ks * 8192 - 65536 + 65536, AUX + t0b + ks * 8192); pin(fuv[ks]); }
        vm0();
    } else {
        if (wave == 7) glds(ws + WS_EPOCH + lane * 16, EPL, 2);
        if (wave == 6) { const char* tp = lane < 8 ? ws + WS_FLAG + lane * 1024 + (a >> 4) * 128 : ws + WS_QA + (((lane & 1) * 32 + hh) * 512 + 128 * rr + ((lane >> 1) & 1) * 64) * 2; glds(tp, EPL + 3072, 2); }
        for (int n = wave; n < 32; n += 8) { int row = 4 * n + g; glds(wuk + ((size_t)(128 * rr + row) * 32 + hh) * 128 + ((l15 ^ sw(row)) << 3), WKI + n * 1024, 1); }
        { v4u s4 = {SENT, SENT, SENT, SENT};
          st16(rs, WS_CLR + a * CLR_PER + wave * 1024 + lane * 16, s4);
          st16(rs, WS_CLR + a * CLR_PER + 8192 + (wave < 5 ? wave : 0) * 1024 + lane * 16, s4); }
#pragma unroll
        for (int ks = 0; ks < 4; ++ks) fuv[ks] = bf16x8{0, 0, 0, 0, 0, 0, 0, 0};
#pragma unroll
        for (int i = 0; i < NA2 / 8; ++i) dma_kv(wave + 8 * i);
        unsigned e1r = WS_E1 + ((hh * 4 + g) * 2 + (l15 >> 3)) * 1024 + (16 * wave + 2 * (lane & 7)) * 8;
        unsigned qw = QIMG + (l15 >> 3) * 272 + (16 * wave + 2 * (lane & 7)) * 2;
        unsigned ka0, ka1, qr, qaw;
        { const int c0 = 32 * (wave & 3) + 8 * q4 + (l15 & 3), c1 = c0 + 4; ka0 = WKI + c0 * 256 + ((g ^ sw(c0)) << 4); ka1 = WKI + c1 * 256 + ((g ^ sw(c1)) << 4);
          qr = QIMG + b1 * 272 + 16 * g; qaw = WS_QA + ((b1 * 32 + hh) * 512 + 128 * rr + 32 * (wave & 3) + 8 * g) * 2; }
        pin(e1r); pin(qw); pin(ka0); pin(ka1); pin(qr); pin(qaw);
        vm0();
        bar();
        { v2u ep = *(LAS v2u*)(L + EPL); u64 t = (((u64)ep[1] << 32) | ep[0]) + 1ull; tag0 = (unsigned)t; tag1 = (unsigned)(t >> 32); }
        const unsigned t40lo = tag0 << 24, t40hi = (tag0 >> 8) | (tag1 << 24);
        { v4u v; bool okv = false;
          for (int sp = 0; sp < SPIN_CAP; ++sp) { asm volatile("" ::: "memory");
              if (!okv) v = ld16(rs, e1r);
              okv = (v[1] == t40hi) & (v[3] == t40hi) & ((v[0] & 0xFF000000u) == t40lo) & ((v[2] & 0xFF000000u) == t40lo);
              if (__all(okv)) break; if (E1_SLEEP) __builtin_amdgcn_s_sleep(E1_SLEEP); }
          float q0 = sum32(sum16(__uint_as_float(v[0] << 8))), q1 = sum32(sum16(__uint_as_float(v[2] << 8)));
          if (lane < 16) *(LAS unsigned*)(L + qw) = pk2(q0, q1);
        }
        bar();
        if (wave < 4) {
            f32x4 d0 = z4, d1 = z4;
#pragma unroll
            for (int ks = 0; ks < 4; ++ks) { bf16x8 bq = *(LAS bf16x8*)(L + qr + 64 * ks);
                d0 = mfma16(*(LAS bf16x8*)(L + (ka0 ^ (ks << 6))), bq, d0); d1 = mfma16(*(LAS bf16x8*)(L + (ka1 ^ (ks << 6))), bq, d1); }
            if (l15 < 2) st16(rs, qaw, v4u{pk2(d0[0], d0[1]), pk2(d0[2], d0[3]), pk2(d1[0], d1[1]), pk2(d1[2], d1[3])});
            vm0();
        }
        bar();
        if (wave == 0 && lane < 8) st8(rs, WS_FLAG + lane * 1024 + a * 8, v2u{tag0, tag1});
        if (wave >= 4) for (int r = NA2 + wave - 4; r < NA2 + NB2; r += 4) dma_kv(r);
    }
    const int NAr = isQ ? NA2 : 128, NBr = isQ ? NB2 : 0;
    unsigned a8[8];
#pragma unroll
    for (int j = 0; j < 8; ++j) { a8[j] = AUX + l31 * 1024 + (((2 * j + hf) ^ sw(l31)) << 4); pin(a8[j]); }
    if (wave == 0) {
        for (int sp = 0; sp < SPIN_CAP; ++sp) { asm volatile("" ::: "memory"); v4u f = ld16(rs, WS_FLAG + xc * 1024 + lane * 16);
            if (__all(f[0] == tag0 && f[1] == tag1 && f[2] == tag0 && f[3] == tag1)) break; }
    }
    bar();
    if (wave < 4) for (int r = wave; r < 32; r += 4) glds(ws + WS_QA + (bb * 32 + r) * 1024 + ((lane ^ sw(r)) << 4), AUX + r * 1024, 2);
    bar();
    if (wave < 4) vm0(); else for (int r = NAr + NBr + wave - 4; r < NAr + NBr + NC_ROWS && r < 128; r += 4) dma_kv(r);
    bar();
    if (wave >= 4) for (int r = NAr + NBr + NC_ROWS + wave - 4; r < 128; r += 4) dma_kv(r);
    constexpr unsigned WOI = 32768, PIM = 69632, SCR = 135168, WDI_ = 92160;
    const int af_ = FFN_MAP ? idx * 8 + xc : a;
    const int u0_ = (9 * af_) >> 1, nu_ = ((9 * (af_ + 1)) >> 1) - u0_;
    auto dma_gu = [&](int n) { int q = n * 64 + lane; int row = q / 10, k = q - row * 10; int ku = k >= 5 ? k - 5 : k; if (ku >= nu_) ku = nu_ - 1;
        glds((k >= 5 ? wu : wg) + (size_t)row * FF + (u0_ + ku) * 8, n * 1024, FFN_AUX); };
    auto dma_cw = [&](int n) { glds(wo + (size_t)(hh * 128 + 32 * rr) * MD + n * 512 + lane * 8, WOI + n * 1024, 1); };
    {
        LAS float* mx = (LAS float*)(L + AUX);
        LAS float* lsum = (LAS float*)(L + AUX + 512);
        LAS char* pim = L + AUX + 1024;
        if (wave < 4) {
            bf16x8 qf[32], kf[KD];
#pragma unroll
            for (int s = 0; s < 32; ++s) qf[s] = *(LAS bf16x8*)(L + a8[s & 7] + (s >> 3) * 256);
#pragma unroll
            for (int j = 0; j < 8; ++j) { a8[j] += (unsigned)(wave * 32768) - AUX; pin(a8[j]); }
            unsigned pw = AUX + 1024 + l31 * 272 + (32 * wave + 4 * hf) * 2; pin(pw);
            vm0();
            bar();
            f32x16 s;
#pragma unroll
            for (int i = 0; i < 16; ++i) s[i] = 0.f;
#pragma unroll
            for (int st = 0; st < KD; ++st) { kf[st] = *(LAS bf16x8*)(L + a8[st & 7] + (st >> 3) * 256); SB(); }
#pragma unroll
            for (int st = 0; st < 32 - KD; ++st) { s = mfma32(kf[st % KD], qf[st], s); SB(); kf[st % KD] = *(LAS bf16x8*)(L + a8[(st + KD) & 7] + ((st + KD) >> 3) * 256); SB(); }
#pragma unroll
            for (int st = 32 - KD; st < 32; ++st) s = mfma32(kf[st % KD], qf[st], s);
#pragma unroll
            for (int i = 0; i < 16; i += 2) { unsigned r1 = pk2(s[i], s[i + 1]); unsigned r2 = pk2(lo_f(r1) * 0.08838834764831845f, hi_f(r1) * 0.08838834764831845f);
                s[i] = lo_f(r2); s[i + 1] = hi_f(r2); }
            float m = s[0];
#pragma unroll
            for (int i = 1; i < 16; ++i) m = fmaxf(m, s[i]);
            m = max32(m);
            if (lane < 32) mx[wave * 32 + lane] = m;
            bar();
            const float LOG2E = 1.4426950408889634f;
            const float M = -LOG2E * fmaxf(fmaxf(mx[l31], mx[32 + l31]), fmaxf(mx[64 + l31], mx[96 + l31]));
            float ls = 0.f;
#pragma unroll
            for (int gq = 0; gq < 4; ++gq) { float e0 = ex2(__builtin_fmaf(s[4 * gq], LOG2E, M)), e1 = ex2(__builtin_fmaf(s[4 * gq + 1], LOG2E, M)), e2 = ex2(__builtin_fmaf(s[4 * gq + 2], LOG2E, M)), e3 = ex2(__builtin_fmaf(s[4 * gq + 3], LOG2E, M));
                *(LAS v2u*)(L + pw + 16 * gq) = v2u{pk2(e0, e1), pk2(e2, e3)}; ls += (e0 + e1) + (e2 + e3); }
            ls = sum32(ls);
            if (lane < 32) lsum[wave * 32 + lane] = ls;
            bar();
            if (isQ) { for (int n = wave; n < NF_EARLY; n += 4) dma_gu(n); } else { for (int n = wave; n < NW_EARLY; n += 4) dma_cw(n); }
            bar();
        } else {
            const int cq = wave - 4, blk = g & 1;
            unsigned va[4], vb[4];
#pragma unroll
            for (int t = 0; t < 4; ++t) { const int ra = 8 * hf + q4, rb = ra + 4; int ch = 16 * cq + 4 * t + 2 * blk + (p4 >> 1);
                va[t] = ra * 1024 + ((ch ^ sw(ra)) << 4) + 8 * (p4 & 1); vb[t] = rb * 1024 + ((ch ^ sw(rb)) << 4) + 8 * (p4 & 1); pin(va[t]); pin(vb[t]); }
            unsigned pr = AUX + 1024 + l31 * 272 + 16 * hf, po = WS_PO + ((((bb * 32 + 4 * hf + blk) * 4 + cq) * 128 + split) * 16 + l15) * 16; pin(pr); pin(po);
            vm0();
            bar();
            bf16x8 vf[8][4];
#pragma unroll
            for (int ks = 0; ks < 4; ++ks)
#pragma unroll
                for (int t = 0; t < 4; ++t) vf[ks][t] = trfrag(L, va[t] + ks * 16384, vb[t] + ks * 16384);
#pragma unroll
            for (int t = 0; t < 4; ++t) { va[t] += 65536; vb[t] += 65536; }
#pragma unroll
            for (int ks = 0; ks < VPRE; ++ks)
#pragma unroll
                for (int t = 0; t < 4; ++t) vf[4 + ks][t] = trfrag(L, va[t] + ks * 16384, vb[t] + ks * 16384);
            bar();
#pragma unroll
            for (int ks = VPRE; ks < 4; ++ks)
#pragma unroll
                for (int t = 0; t < 4; ++t) vf[4 + ks][t] = trfrag(L, va[t] + ks * 16384, vb[t] + ks * 16384);
            bar();
            f32x16 o[4];
#pragma unroll
            for (int t = 0; t < 4; ++t)
#pragma unroll
                for (int i = 0; i < 16; ++i) o[t][i] = 0.f;
            bf16x8 pa[8];
#pragma unroll
            for (int ks = 0; ks < 8; ++ks) pa[ks] = *(LAS bf16x8*)(L + pr + 32 * ks);
#pragma unroll
            for (int ks = 0; ks < 8; ++ks)
#pragma unroll
                for (int t = 0; t < 4; ++t) o[t] = mfma32(pa[ks], vf[ks][t], o[t]);
#pragma unroll
            for (int r = 0; r < 16; r += 2) {
                auto x0 = __builtin_amdgcn_permlane16_swap(pk2(o[0][r], o[1][r]), pk2(o[0][r + 1], o[1][r + 1]), false, false);
                auto x1 = __builtin_amdgcn_permlane16_swap(pk2(o[2][r], o[3][r]), pk2(o[2][r + 1], o[3][r + 1]), false, false);
                st16(rs, po + ((r & 3) + 8 * (r >> 2)) * 131072, v4u{x0[0], x1[0], x0[1], x1[1]}); }
            v2u sv = {nosent(fmaxf(fmaxf(mx[l31], mx[32 + l31]), fmaxf(mx[64 + l31], mx[96 + l31]))), nosent((lsum[l31] + lsum[32 + l31]) + (lsum[64 + l31] + lsum[96 + l31]))};
            unsigned so = WS_ST + (((bb * 32 + l31) * 4 + cq) * 128 + split) * 8; pin(sv); pin(so);
            if (!(NODRAIN && isQ)) vm0();
            if (lane < 32) st8(rs, so, sv);
            bar();
        }
    }
    if (isQ == (SWAP != 0)) {
        for (int n = NW_EARLY + wave; n < 36; n += 8) dma_cw(n);
        vm0();
        bar();
        bf16x8 fo[5];
#pragma unroll
        for (int i = 0; i < 5; ++i) { int n = wave + 8 * i; if (n > 35) n = 35; int ra = 8 * g + q4;
            fo[i] = trfrag(L, WOI + ra * 1152 + n * 32 + 8 * p4, WOI + (ra + 4) * 1152 + n * 32 + 8 * p4); pin(fo[i]); }
        constexpr unsigned WIMG = SCR, LINV = SCR + 1280, AIMG = SCR + 1536, AIMG2 = SCR + 2816, R3 = SCR + 3328;
        unsigned pa_ = PIM + t0a, pb_ = PIM + t0b, wr = WIMG + b1 * 272 + 16 * g, aw = AIMG + b1 * 272 + (16 * wave + 4 * g) * 2, ar = AIMG + b1 * 272 + 16 * g;
        unsigned e4w = WS_E4 + ((((hh * 4 + (wave >> 1)) * 4 + rr) * 2 + b1) * 32 + 16 * (wave & 1) + 4 * g) * 4, e4r = WS_E4 + (hh * 4 + rr) * 1024 + lane * 16;
        unsigned a2w = AIMG2 + (l15 >> 3) * 80 + (lane & 7) * 8, a2r = AIMG2 + b1 * 80 + g * 16;
        unsigned e5w = WS_E5 + (((b1 * 18 + (wave >> 1)) * 128 + a) * 32 + 16 * (wave & 1) + 4 * g) * 4;
        pin(pa_); pin(pb_); pin(wr); pin(aw); pin(ar); pin(e4w); pin(e4r); pin(a2w); pin(a2r); pin(e5w);
        v2u sv[2];
        const char* src[4];
        if (tid == 0) *(LAS unsigned*)(L + LINV + 16) = 0u;
        {
#pragma unroll
            for (int k = 0; k < 4; ++k) { int n = wave + 8 * k; int b = n >> 4, row = 64 + 4 * (n & 15) + g; src[k] = ws + WS_PO + ((b * 32 + hh) * 4 + rr) * 32768 + row * 256 + ((l15 ^ sw(row)) << 4); pin(src[k]); }
            if (wave < 2) { const unsigned so_ = WS_ST + (((wave * 32 + hh) * 4 + rr) * 128 + lane) * 8;
                for (int sp = 0; sp < SPIN_CAP; ++sp) { asm volatile("" ::: "memory"); sv[0] = ld8(rs, so_); sv[1] = ld8(rs, so_ + 512); if (__all(sv[0][0] != SENT && sv[0][1] != SENT && sv[1][0] != SENT && sv[1][1] != SENT)) break; }
            }
            bar();
#pragma unroll
            for (int k = 0; k < 4; ++k) { int n = wave + 8 * k; glds(src[k] - 16384, PIM + (n >> 4) * 32768 + (n & 15) * 1024, 2); }
#pragma unroll
            for (int k = 0; k < 4; ++k) { int n = wave + 8 * k; glds(src[k], PIM + (n >> 4) * 32768 + (16 + (n & 15)) * 1024, 2); }
        }
        if (wave < 2) { float m0 = __uint_as_float(sv[0][0]), m1 = __uint_as_float(sv[1][0]);
            float M = max32(max16(rowmax(fmaxf(m0, m1))));
            unsigned wp = pk2(ex2((m0 - M) * 1.4426950408889634f), ex2((m1 - M) * 1.4426950408889634f));
            float Ls = sum32(halfsum(lo_f(wp) * __uint_as_float(sv[0][1]) + hi_f(wp) * __uint_as_float(sv[1][1])));
            *(LAS u16*)(L + WIMG + wave * 272 + lane * 2) = (u16)wp; *(LAS u16*)(L + WIMG + wave * 272 + 128 + lane * 2) = (u16)(wp >> 16);
            if (lane == 0) *(LAS float*)(L + LINV + wave * 4) = __builtin_amdgcn_rcpf(Ls); }
        for (unsigned it = 1;; ++it) {
            vm0();
            bar();
            f32x4 d0 = z4, d1 = z4;
            typedef unsigned short us2 __attribute__((ext_vector_type(2)));
            us2 mxu = {0, 0};
#pragma unroll
            for (int ks = 0; ks < 4; ++ks) { bf16x8 bw = *(LAS bf16x8*)(L + wr + 64 * ks);
                bf16x8 f0 = trfrag(L, pa_ + ks * 8192, pb_ + ks * 8192), f1 = trfrag(L, pa_ + 32768 + ks * 8192, pb_ + 32768 + ks * 8192);
                d0 = mfma16(f0, bw, d0); d1 = mfma16(f1, bw, d1);
                if (NODRAIN && ks >= 2) { v4u u0 = __builtin_bit_cast(v4u, f0), u1 = __builtin_bit_cast(v4u, f1);
#pragma unroll
                    for (int j = 0; j < 4; ++j) { mxu = __builtin_elementwise_max(mxu, __builtin_bit_cast(us2, u0[j])); mxu = __builtin_elementwise_max(mxu, __builtin_bit_cast(us2, u1[j])); } } }
            const float inv = *(LAS float*)(L + LINV + b1 * 4);
            f32x4 r = (b1 ? d1 : d0) * inv;
            if (l15 < 2) *(LAS v2u*)(L + aw) = v2u{pk2(r[0], r[1]), pk2(r[2], r[3])};
            if (NODRAIN && (mxu[0] == 0xFFFFu || mxu[1] == 0xFFFFu)) *(LAS unsigned*)(L + LINV + 16) = it;
            bar();
            if (!NODRAIN) break;
            if (__builtin_amdgcn_readfirstlane(*(LAS unsigned*)(L + LINV + 16)) != it || it > 2000u) break;
#pragma unroll
            for (int k = 0; k < 4; ++k) { int n = wave + 8 * k; glds(src[k], PIM + (n >> 4) * 32768 + (16 + (n & 15)) * 1024, 2); }
        }
        { f32x4 d = z4;
#pragma unroll
          for (int ks = 0; ks < 4; ++ks) d = mfma16(fuv[ks], *(LAS bf16x8*)(L + ar + 64 * ks), d);
          if (l15 < 2) st16(rs, e4w, nos4(d)); }
        if (wave == 0) { v4u v;
            for (int sp = 0; sp < SPIN_CAP; ++sp) { asm volatile("" ::: "memory"); v = ld16(rs, e4r); if (__all(ok4(v))) break; }
            float c[4];
#pragma unroll
            for (int j = 0; j < 4; ++j) c[j] = sum32(sum16(__uint_as_float(v[j])));
            if (lane < 16) *(LAS v2u*)(L + a2w) = v2u{pk2(c[0], c[1]), pk2(c[2], c[3])}; }
        bar();
        { bf16x8 av = *(LAS bf16x8*)(L + a2r);
          f32x4 d[5];
#pragma unroll
          for (int i = 0; i < 5; ++i) d[i] = mfma16(fo[i], av, z4);
          if (l15 < 2) {
#pragma unroll
              for (int i = 0; i < 4; ++i) st16(rs, e5w + i * 65536, nos4(d[i]));
              if (wave < 4) st16(rs, e5w + 4 * 65536, nos4(d[4])); } }
        const int rho = xc + 8 * idx;
        if (rho < 36) {
            LAS float* r3 = (LAS float*)(L + R3);
            f32x4 hv;
            const v2u xv = *(const v2u*)(x + rho * 32 + (lane & 7) * 4);
#pragma unroll
            for (int ph = 0; ph < 2; ++ph) {
                const unsigned src = (ph ? WS_E7 : WS_E5) + rho * 16384 + tid * 16;
                v4u v0, v1;
                bool k0 = false, k1 = false;
                for (int sp = 0; sp < SPIN_CAP; ++sp) { asm volatile("" ::: "memory"); if (!k0) v0 = ld16(rs, src); if (!k1) v1 = ld16(rs, src + 8192); k0 = ok4(v0); k1 = ok4(v1); if (__all(k0 && k1)) break; if (R_SLEEP) __builtin_amdgcn_s_sleep(R_SLEEP); }
                f32x4 s;
#pragma unroll
                for (int j = 0; j < 4; ++j) { float t = __uint_as_float(v0[j]) + __uint_as_float(v1[j]); t += dpp<0x128>(t); s[j] = sum32(sum16(t)); }
                if (lane < 8) *(LAS f32x4*)(r3 + wave * 32 + lane * 4) = s;
                bar();
                if (wave == 0) { const int q = lane & 7; f32x4 t = z4;
#pragma unroll
                    for (int w = 0; w < 8; ++w) t += *(LAS f32x4*)(r3 + w * 32 + q * 4);
                    if (ph == 0) {
                        { unsigned a0 = pk2(t[0], t[1]), a1 = pk2(t[2], t[3]); hv = f32x4{lo_f(a0) + lo_f(xv[0]), hi_f(a0) + hi_f(xv[0]), lo_f(a1) + lo_f(xv[1]), hi_f(a1) + hi_f(xv[1])}; }
                        unsigned h0 = pk2(hv[0], hv[1]), h1 = pk2(hv[2], hv[3]);
                        hv = f32x4{lo_f(h0), hi_f(h0), lo_f(h1), hi_f(h1)};
                        st8(rs, WS_H + (lane >> 3) * 8192 + rho * 64 + q * 8, v2u{__builtin_elementwise_min(h0, 0xFFFFFFFEu), __builtin_elementwise_min(h1, 0xFFFFFFFEu)});
                        f32x4 r = {lo_f(h0), hi_f(h0), lo_f(h1), hi_f(h1)};
                        float sm = (r[0] + r[1]) + (r[2] + r[3]); sm += dpp<0xB1>(sm); sm += dpp<0x4E>(sm); sm += dpp<0x141>(sm);
                        r -= sm * (1.f / 32.f);
                        float m2 = (r[0] * r[0] + r[1] * r[1]) + (r[2] * r[2] + r[3] * r[3]); m2 += dpp<0xB1>(m2); m2 += dpp<0x4E>(m2); m2 += dpp<0x141>(m2);
                        if (q == 0) st8(rs, WS_H + (lane >> 3) * 8192 + 4096 + rho * 8, v2u{nosent(sm), nosent(m2)});
                        if (rho == 0 && lane == 0) st8(rs, WS_EPOCH, v2u{tag0, tag1});
                    } else if (lane < 8) { unsigned a0 = pk2(t[0], t[1]), a1 = pk2(t[2], t[3]); *(v2u*)(out + rho * 32 + q * 4) = v2u{pk2(hv[0] + lo_f(a0), hv[1] + hi_f(a0)), pk2(hv[2] + lo_f(a1), hv[3] + hi_f(a1))}; } }
                bar();
            }
        }
    } else {
        const int u0 = u0_, nu = nu_;
        constexpr unsigned WDI = 92160;
        if (FFN_SLEEP) __builtin_amdgcn_s_sleep(FFN_SLEEP);
        {
            for (int n = NF_EARLY + wave; n < 90; n += 8) dma_gu(n);
            for (int n = wave; n < 9 * nu; n += 8) glds(wd + (size_t)u0 * 8 * MD + n * 512 + lane * 8, WDI + n * 1024, 1);
        }
        vm0();
        bar();
        bf16x8 fg[3][5], fd[5][2];
#pragma unroll
        for (int kk = 0; kk < 3; ++kk) { int ks = wave + 8 * kk; if (ks > 17) ks = 17; int ra = 32 * ks + 8 * g + q4;
#pragma unroll
            for (int t = 0; t < 5; ++t) { fg[kk][t] = trfrag(L, ra * 160 + t * 32 + 8 * p4, (ra + 4) * 160 + t * 32 + 8 * p4); pin(fg[kk][t]); } }
#pragma unroll
        for (int i = 0; i < 5; ++i) { int n = wave + 8 * i; if (n > 35) n = 35; int ra = 8 * g + q4;
            fd[i][0] = trfrag(L, WDI + ra * 1152 + n * 32 + 8 * p4, WDI + (ra + 4) * 1152 + n * 32 + 8 * p4);
            int rc = 32 + q4;
            bf16x8 f1 = trfrag(L, WDI + rc * 1152 + n * 32 + 8 * p4, WDI + (rc + 4) * 1152 + n * 32 + 8 * p4);
            if (g != 0 || nu < 5) f1 = bf16x8{0, 0, 0, 0, 0, 0, 0, 0};
            fd[i][1] = f1; pin(fd[i][0]); pin(fd[i][1]); }
        constexpr unsigned RED2 = 144384, AIMG = 149504, STS = 150528;
        unsigned hsrc = WS_H + xc * 8192 + b1 * 1152 + (32 * wave + 8 * g) * 2, ssrc = WS_H + xc * 8192 + 4096 + ((wave & 1) * 18 + (l31 < 18 ? l31 : 0)) * 8;
        unsigned r2w = RED2 + ((wave * 2 + b1) * 80 + 4 * g) * 4, r2r = RED2 + ((wave & 1) * 80 + (lane < 40 ? lane : 0)) * 4, acw = AIMG + (wave & 1) * 144 + lane * 2, acr = AIMG + b1 * 144 + g * 16;
        unsigned e7w = WS_E7 + (((b1 * 18 + (wave >> 1)) * 128 + a) * 32 + 16 * (wave & 1) + 4 * g) * 4;
        float amask = lane < 8 * nu ? 1.f : 0.f, ok18 = l31 < 18 ? 1.f : 0.f;
        float csg = 0.f, csu = 0.f;
        {
            const bf16x8 ones = asb(v4u{0x3F803F80u, 0x3F803F80u, 0x3F803F80u, 0x3F803F80u});
            f32x4 d[5];
#pragma unroll
            for (int t = 0; t < 5; ++t) { d[t] = mfma16(fg[1][t], ones, mfma16(fg[0][t], ones, z4)); if (wave < 2) d[t] = mfma16(fg[2][t], ones, d[t]); }
            if (l15 < 2) {
#pragma unroll
                for (int t = 0; t < 5; ++t) *(LAS f32x4*)(L + r2w + 64 * t) = d[t]; }
            bar();
#pragma unroll
            for (int w = 0; w < 8; ++w) { csg += *(LAS float*)(L + r2r + w * 640); csu += *(LAS float*)(L + r2r + w * 640 + 160); }
            bar();
        }
        pin(hsrc); pin(ssrc); pin(r2w); pin(r2r); pin(acw); pin(acr); pin(e7w); pin(amask); pin(ok18); pin(csg); pin(csu);
        { v4u hv[3]; v2u sv;
          for (int sp = 0; sp < SPIN_CAP; ++sp) { asm volatile("" ::: "memory");
              hv[0] = ld16(rs, hsrc); hv[1] = ld16(rs, hsrc + 512); bool ok = ok4(hv[0]) && ok4(hv[1]);
              if (wave < 2) { hv[2] = ld16(rs, hsrc + 1024); ok = ok && ok4(hv[2]); }
              if (wave >= 6) { sv = ld8(rs, ssrc); ok = ok && sv[0] != SENT && sv[1] != SENT; }
              if (__all(ok)) break; __builtin_amdgcn_s_sleep(H_SLEEP); }
          f32x4 d[5];
#pragma unroll
          for (int t = 0; t < 5; ++t) d[t] = mfma16(fg[0][t], asb(hv[0]), z4);
#pragma unroll
          for (int t = 0; t < 5; ++t) d[t] = mfma16(fg[1][t], asb(hv[1]), d[t]);
          if (wave < 2) {
#pragma unroll
              for (int t = 0; t < 5; ++t) d[t] = mfma16(fg[2][t], asb(hv[2]), d[t]); }
          if (wave >= 6) { float sm = __uint_as_float(sv[0]) * ok18; float mu = halfsum(sm) * (1.f / 576.f); float dm = sm * (1.f / 32.f) - mu;
              float var = halfsum((__uint_as_float(sv[1]) + 32.f * dm * dm) * ok18) * (1.f / 576.f);
              if (lane == 0) *(LAS v2u*)(L + STS + (wave & 1) * 8) = v2u{__float_as_uint(rb(mu)), __float_as_uint(__builtin_amdgcn_rcpf(rb(__builtin_amdgcn_sqrtf(rb(rb(var) + 1e-5f)))))}; }
          if (l15 < 2) {
#pragma unroll
              for (int t = 0; t < 5; ++t) *(LAS f32x4*)(L + r2w + 64 * t) = d[t]; } }
        bar();
        if (wave < 2) {
            float gs = 0.f, us = 0.f;
            const v2u st = *(LAS v2u*)(L + STS + wave * 8); const float mu = __uint_as_float(st[0]), rsd = __uint_as_float(st[1]);
#pragma unroll
            for (int w = 0; w < 8; ++w) { gs += *(LAS float*)(L + r2r + w * 640); us += *(LAS float*)(L + r2r + w * 640 + 160); }
            gs = rb((gs - mu * csg) * rsd); us = rb((us - mu * csu) * rsd);
            float act = rb(gs * __builtin_amdgcn_rcpf(1.f + __expf(-gs))) * us;
            act = amask != 0.f ? act : 0.f;
            *(LAS u16*)(L + acw) = (u16)pk2(act, 0.f); }
        bar();
        { bf16x8 a0 = *(LAS bf16x8*)(L + acr), a1 = *(LAS bf16x8*)(L + acr + 64);
          f32x4 d[5];
#pragma unroll
          for (int i = 0; i < 5; ++i) d[i] = mfma16(fd[i][0], a0, z4);
#pragma unroll
          for (int i = 0; i < 5; ++i) d[i] = mfma16(fd[i][1], a1, d[i]);
          if (l15 < 2) {
#pragma unroll
              for (int i = 0; i < 4; ++i) st16(rs, e7w + i * 65536, nos4(d[i]));
              if (wave < 4) st16(rs, e7w + 4 * 65536, nos4(d[4])); } }
    }
}

extern "C" void kernel_launch(void* const* d_in, const int* in_sizes, int n_in, void* d_out, int out_size, void* d_ws, size_t ws_size, hipStream_t stream) {
    (void)hipFuncSetAttribute((const void*)mk, hipFuncAttributeMaxDynamicSharedMemorySize, LDS_BYTES);
    mk<<<256, NT, LDS_BYTES, stream>>>((const u16*)d_in[0], (const u16*)d_in[1], (const u16*)d_in[2], (const u16*)d_in[3], (const u16*)d_in[4], (const u16*)d_in[5],
                                       (const u16*)d_in[7], (const u16*)d_in[8], (const u16*)d_in[9], (u16*)d_out, (char*)d_ws);
}
```
